```python
import torch

def _softmax(x, axis=-1):
    return torch.softmax(x, dim=axis)
def _gelu(x):
    return torch.nn.functional.gelu(x, approximate="tanh")
def _silu(x):
    return torch.nn.functional.silu(x)
def _sigmoid(x):
    return torch.sigmoid(x)
def _layernorm(x, axis=-1, eps=1e-5):
    m = x.mean(dim=axis, keepdim=True)
    v = x.var(dim=axis, keepdim=True, unbiased=False)
    return (x - m) / torch.sqrt(v + eps)
def _scatter_add(tgt, idx, src, axis=0):
    return tgt.clone().index_add_(axis, idx, src)
def _segment_sum(src, seg, n):
    out = torch.zeros((n,) + src.shape[1:], dtype=src.dtype)
    return out.index_add_(0, seg, src)
def _pad(x, pw):
    flat = tuple(v for pair in reversed(pw) for v in pair)
    return torch.nn.functional.pad(x, flat)

kernel_name = 'gated_mix__linear_attn_block__mixer_s_mlp__prompt'

def setup_inputs():

    _w = (lambda a: (lambda t: t.to(torch.bfloat16) if t.is_floating_point() else t)(torch.as_tensor(a)))
    def _A_setup():
        B, S, D = 1, 2048, 1024
        return {
            "x": _w((lambda *s: torch.randn(*s, dtype=torch.bfloat16) * 0.05)(B, S, D)),
            "wqkv": _w((lambda *s: torch.randn(*s, dtype=torch.bfloat16) * 3.2)(D, 3 * D)),
            "wo": _w((lambda *s: torch.randn(*s, dtype=torch.bfloat16) * 1.6)(D, D)),
        }
    _A_in = _A_setup()

    def _B_setup():
        B, S, D, F = 1, 2048, 1024, 3584
        return {
            "x":  _w((lambda *s: torch.randn(*s, dtype=torch.bfloat16) * 0.05)(B, S, D)),
            "w1": _w((lambda *s: torch.randn(*s, dtype=torch.bfloat16) * 3.2)(D, D) * (D ** -0.5)),
            "w2": _w((lambda *s: torch.randn(*s, dtype=torch.bfloat16) * 3.2)(D, D) * (D ** -0.5)),
            "w3": _w((lambda *s: torch.randn(*s, dtype=torch.bfloat16) * 3.2)(D, F) * (D ** -0.5)),
            "w4": _w((lambda *s: torch.randn(*s, dtype=torch.bfloat16) * 1.6)(F, D) * (F ** -0.5)),
        }
    _B_in = _B_setup()

    return {
        "A_x": _A_in["x"],
        "A_wqkv": _A_in["wqkv"],
        "A_wo": _A_in["wo"],
        "B_x": _B_in["x"],
        "B_w1": _B_in["w1"],
        "B_w2": _B_in["w2"],
        "B_w3": _B_in["w3"],
        "B_w4": _B_in["w4"],
        "gate_w": _w((lambda *s: torch.randn(*s, dtype=torch.bfloat16) * 0.05)(1024) * 16.0),
    }

def reference(A_x, A_wqkv, A_wo, B_x, B_w1, B_w2, B_w3, B_w4, gate_w):
    def _A_ref(x, wqkv, wo):
        B, S, D = x.shape
        H = 32; Dh = D // H
        qkv = (lambda x, *d: x.permute(*d))((x @ wqkv).reshape(B, S, 3, H, Dh), 2, 0, 3, 1, 4)
        q, k, v = qkv[0], qkv[1], qkv[2]
        phi = lambda t: torch.where(t > 0, t + 1, torch.exp(t)) + 1e-6
        pq, pk = phi(q), phi(k)
        kv = torch.einsum("bhsd,bhse->bhde", pk, v)
        z = torch.einsum("bhsd->bhd", pk)
        num = torch.einsum("bhqd,bhde->bhqe", pq, kv)
        den = torch.einsum("bhqd,bhd->bhq", pq, z)[..., None] + 1e-6
        o = num / den
        return (lambda x, *d: x.permute(*d))(o, 0, 2, 1, 3).reshape(B, S, D) @ wo

    def _B_ref(x, w1, w2, w3, w4):
        B, S, D = x.shape
        h = _gelu(_layernorm(x, axis=-1) @ w1)
        c = D // 4
        g0 = _pad(h[:, :, 0*c:1*c], ((0, 0), (1, 0), (0, 0)))[:, :S, :]
        g1 = _pad(h[:, :, 1*c:2*c], ((0, 0), (0, 1), (0, 0)))[:, 1:, :]
        g2 = _pad(h[:, :, 2*c:3*c], ((0, 0), (2, 0), (0, 0)))[:, :S, :]
        g3 = _pad(h[:, :, 3*c:   ], ((0, 0), (0, 2), (0, 0)))[:, 2:, :]
        hs = torch.concatenate([g0, g1, g2, g3], axis=-1) @ w2
        x1 = x + hs
        h2 = _gelu(_layernorm(x1, axis=-1) @ w3) @ w4
        return x1 + h2

    x = A_x
    g = _sigmoid((x * gate_w).sum(-1, keepdims=True))
    a = _A_ref(x, A_wqkv, A_wo)
    b = _B_ref(x, B_w1, B_w2, B_w3, B_w4)
    return g * a + (1 - g) * b
```

```cpp
#include <hip/hip_runtime.h>

typedef unsigned short u16;
typedef unsigned long long u64;
typedef short bf16x8 __attribute__((ext_vector_type(8)));
typedef short s16x4 __attribute__((ext_vector_type(4)));
typedef float f32x4 __attribute__((ext_vector_type(4)));
typedef float f32x2 __attribute__((ext_vector_type(2)));
typedef unsigned u32x4 __attribute__((ext_vector_type(4)));
typedef unsigned u32x2 __attribute__((ext_vector_type(2)));
typedef __bf16 bf16x2_t __attribute__((ext_vector_type(2)));
#define LAS __attribute__((address_space(3)))
typedef LAS char* lds_t;
typedef u32x4 rsrc_t;
#define DI __device__ __forceinline__

constexpr int S_ = 2048, D_ = 1024, F_ = 3584;
constexpr int LDAU = 4608 + 64;
constexpr size_t MB = 1u << 20;
constexpr size_t WS_HS = 0, WS_PQ = 4 * MB, WS_X1 = 8 * MB, WS_AU = 12 * MB, WS_KVP = 32 * MB, WS_ZP = 36 * MB,
                 WS_KVF = 36 * MB + 512 * 1024, WS_ZF = 36 * MB + 768 * 1024, WS_ST = 37 * MB, WS_G = 37 * MB + 512 * 1024,
                 WS_FLAG = 38 * MB, WS_XCH = 40 * MB;

DI rsrc_t mk_rsrc(const void* p, unsigned bytes) {
  u64 a = (u64)p; rsrc_t r; r.x = (unsigned)a; r.y = (unsigned)(a >> 32) & 0xffffu; r.z = bytes; r.w = 0x00020000u; return r;
}
DI void glds16(rsrc_t r, lds_t dst, unsigned voff, unsigned soff) {
  asm volatile("s_mov_b32 m0, %0\n\ts_nop 0\n\tbuffer_load_dwordx4 %1, %2, %3 offen lds" ::"s"((unsigned)(size_t)dst), "v"(voff), "s"(r), "s"(soff) : "memory");
}
template <int N> DI void wait_vm() { asm volatile("s_waitcnt vmcnt(%0)" ::"n"(N) : "memory"); }
DI void wait_lgkm0() { asm volatile("" ::: "memory"); __builtin_amdgcn_s_waitcnt(0xC07F); asm volatile("" ::: "memory"); }
DI void bar() { asm volatile("" ::: "memory"); __builtin_amdgcn_s_barrier(); asm volatile("" ::: "memory"); }
DI unsigned cvt_pk(float lo, float hi) { f32x2 v = {lo, hi}; bf16x2_t r = __builtin_convertvector(v, bf16x2_t); return __builtin_bit_cast(unsigned, r); }
DI float bf_lo(unsigned u) { return __builtin_bit_cast(float, u << 16); }
DI float bf_hi(unsigned u) { return __builtin_bit_cast(float, u & 0xffff0000u); }
DI float rbf(float f) { return bf_lo(cvt_pk(f, 0.f) & 0xffffu); }
DI float dot2(unsigned a, unsigned b, float c) { return __builtin_amdgcn_fdot2_f32_bf16(__builtin_bit_cast(bf16x2_t, a), __builtin_bit_cast(bf16x2_t, b), c, false); }
constexpr unsigned ONES = 0x3f803f80u;
typedef __amdgpu_buffer_rsrc_t brs_t;
constexpr unsigned WS_BYTES = 48u << 20;
DI brs_t mk_brs(void* p, unsigned bytes) { return __builtin_amdgcn_make_buffer_rsrc(p, (short)0, (int)bytes, 0x00020000); }
DI void st16(brs_t r, unsigned off, u32x4 v) { __builtin_amdgcn_raw_buffer_store_b128(v, r, (int)off, 0, 16); }
DI void st8(brs_t r, unsigned off, u32x2 v) { __builtin_amdgcn_raw_buffer_store_b64(v, r, (int)off, 0, 16); }
DI void st16f(brs_t r, unsigned off, f32x4 v) { st16(r, off, __builtin_bit_cast(u32x4, v)); }
DI float sum_g(float v) {
  unsigned u = __builtin_bit_cast(unsigned, v);
  auto a = __builtin_amdgcn_permlane16_swap(u, u, false, false);
  const unsigned a0 = a[0], a1 = a[1];
  v = __uint_as_float(a0) + __uint_as_float(a1);
  u = __builtin_bit_cast(unsigned, v);
  auto b = __builtin_amdgcn_permlane32_swap(u, u, false, false);
  const unsigned b0 = b[0], b1 = b[1];
  return __uint_as_float(b0) + __uint_as_float(b1);
}
DI float gelu_t(float v) {
  const float e = __builtin_amdgcn_exp2f(v * __builtin_fmaf(v * v, -0.10294323970f, -2.30220819814f));
  return v * __builtin_amdgcn_rcpf(1.f + e);
}
DI float phi_f(float t) { return (t > 0.f ? t + 1.f : __builtin_amdgcn_exp2f(t * 1.44269504089f)) + 1e-6f; }
DI u32x4 pair_swap(unsigned a0, unsigned a1, unsigned b0, unsigned b1) {
  auto r0 = __builtin_amdgcn_permlane16_swap(a0, b0, false, false);
  auto r1 = __builtin_amdgcn_permlane16_swap(a1, b1, false, false);
  u32x4 v; v.x = r0[0]; v.y = r1[0]; v.z = r0[1]; v.w = r1[1]; return v;
}

template <int NWB_, int NNB_, int NT_, int NS_>
struct Cfg {
  static constexpr int NWB = NWB_, NNB = NNB_, NT = NT_, NS = NS_, MT = 4;
  static constexpr int A_BYTES = 16384, STAGE = A_BYTES + NWB * 8192 + NNB * 4096, XTRA = NS * STAGE, LDS = XTRA + 4096;
  static constexpr int L = 4 + 2 * NWB + NNB;
};
template <class C> struct Frags { bf16x8 a[C::MT]; bf16x8 b[C::NT]; };

DI bf16x8 rd_bfrag(lds_t p, int hi_off) {
  s16x4 lo = __builtin_amdgcn_ds_read_tr16_b64_v4i16((LAS s16x4*)p);
  s16x4 hi = __builtin_amdgcn_ds_read_tr16_b64_v4i16((LAS s16x4*)(p + hi_off));
  return __builtin_shufflevector(lo, hi, 0, 1, 2, 3, 4, 5, 6, 7);
}
template <class C, class P>
DI void read_frags(Frags<C>& F, lds_t st, int ks, unsigned aoff, const unsigned (&boff)[P::NBASE]) {
#pragma unroll
  for (int i = 0; i < C::NT; ++i)
    F.b[i] = P::narrow(i) ? rd_bfrag(st + boff[P::base(i)] + ks * 2048, 256) : rd_bfrag(st + boff[P::base(i)] + P::imm(i) + ks * 4096, 512);
#pragma unroll
  for (int m = 0; m < C::MT; ++m) F.a[m] = *(const LAS bf16x8*)(st + aoff + m * 2048);
}
template <class C, class P>
DI void mma(f32x4 (&acc)[C::MT][C::NT], const Frags<C>& F) {
#pragma unroll
  for (int n = 0; n < C::NT; ++n)
#pragma unroll
    for (int m = 0; m < C::MT; ++m)
      acc[m][n] = P::trans(n) ? __builtin_amdgcn_mfma_f32_16x16x32_bf16(F.b[n], F.a[m], acc[m][n], 0, 0, 0)
                              : __builtin_amdgcn_mfma_f32_16x16x32_bf16(F.a[m], F.b[n], acc[m][n], 0, 0, 0);
}
template <class C>
DI void sched_hint() {
#pragma unroll
  for (int i = 0; i < C::MT * C::NT; ++i) {
    __builtin_amdgcn_sched_group_barrier(0x8, 1, 0);
    if (i < C::MT + 2 * C::NT) __builtin_amdgcn_sched_group_barrier(0x100, 1, 0);
  }
  __builtin_amdgcn_sched_barrier(0);
}
template <class C, class P>
DI void consume(lds_t smem, int nt, f32x4 (&acc)[C::MT][C::NT], int wm, int lane, const unsigned (&boff)[P::NBASE]) {
  const int fi = lane & 15, g = lane >> 4, f = (fi >> 1) & 7;
  const unsigned aoff0 = (unsigned)((wm * 64 + fi) * 128 + ((g ^ f) * 16));
  const unsigned aoff1 = (unsigned)((wm * 64 + fi) * 128 + (((4 + g) ^ f) * 16));
#pragma unroll
  for (int m = 0; m < C::MT; ++m)
#pragma unroll
    for (int n = 0; n < C::NT; ++n) acc[m][n] = (f32x4){0.f, 0.f, 0.f, 0.f};
  Frags<C> F0, F1;
  bar();
  read_frags<C, P>(F0, smem, 0, aoff0, boff);
  int rs = 0;
  for (int t = 0; t < nt - 1; ++t) {
    wait_lgkm0();
    __builtin_amdgcn_sched_barrier(0);
    read_frags<C, P>(F1, smem + rs * C::STAGE, 1, aoff1, boff);
    mma<C, P>(acc, F0);
    sched_hint<C>();
    rs = (rs + 1 == C::NS) ? 0 : rs + 1;
    wait_lgkm0();
    bar();
    __builtin_amdgcn_sched_barrier(0);
    read_frags<C, P>(F0, smem + rs * C::STAGE, 0, aoff0, boff);
    mma<C, P>(acc, F1);
    sched_hint<C>();
  }
  wait_lgkm0();
  __builtin_amdgcn_sched_barrier(0);
  read_frags<C, P>(F1, smem + rs * C::STAGE, 1, aoff1, boff);
  mma<C, P>(acc, F0);
  sched_hint<C>();
  mma<C, P>(acc, F1);
}
template <class C, class LD>
DI void produce(lds_t smem, int nt, LD& L) {
#pragma unroll
  for (int s = 0; s < C::NS - 1; ++s) L.issue(smem + s * C::STAGE, s);
  wait_vm<(C::NS - 2) * C::L>();
  bar();
  int ws = C::NS - 1, rs = 0;
  for (int t = 0; t < nt - 1; ++t) {
    const bool more = t + C::NS - 1 < nt;
    if (more) L.issue(smem + ws * C::STAGE, t + C::NS - 1);
    L.side(smem + rs * C::STAGE);
    if (more) wait_vm<(C::NS - 2) * C::L>();
    else if (C::NS >= 4 && t + C::NS - 2 < nt) wait_vm<(C::NS >= 4 ? C::NS - 3 : 0) * C::L>();
    else wait_vm<0>();
    ws = (ws + 1 == C::NS) ? 0 : ws + 1; rs = (rs + 1 == C::NS) ? 0 : rs + 1;
    wait_lgkm0();
    bar();
  }
  L.side(smem + rs * C::STAGE);
}
DI unsigned voff_a(int lane, int lw, unsigned lda2) { return (unsigned)(lane >> 3) * lda2 + (unsigned)(((lane & 7) ^ (((lw & 1) * 4 + (lane >> 4)) & 7)) * 16); }
DI unsigned voff_w(int lane, int lw, unsigned ldb2) { return (unsigned)(lane >> 3) * ldb2 + (unsigned)(((lane & 7) ^ (((lane >> 4) & 1) * 2 + (lw & 1) * 4)) * 16); }
DI unsigned voff_n(int lane, unsigned ldb2) { return (unsigned)(lane >> 2) * ldb2 + (unsigned)(((lane & 3) ^ (((lane >> 5) & 1) * 2)) * 16); }
DI void issue_a(rsrc_t rA, lds_t st, int lw, unsigned voff, unsigned soff  , unsigned lda2) {
#pragma unroll
  for (int i = 0; i < 4; ++i) glds16(rA, st + (lw + 4 * i) * 1024, voff, soff + (unsigned)(lw + 4 * i) * 8u * lda2);
}
DI void issue_w(rsrc_t rB, lds_t blk, int lw, unsigned voff, unsigned soff  , unsigned ldb2) {
#pragma unroll
  for (int i = 0; i < 2; ++i) glds16(rB, blk + (lw + 4 * i) * 1024, voff, soff + (unsigned)(lw + 4 * i) * 8u * ldb2);
}
DI unsigned roff_w(int lane, int j) {
  const int g = lane >> 4, q = (lane >> 2) & 3, pp = lane & 3, f = ((q >> 1) & 1) | ((g & 1) << 1);
  return (unsigned)((8 * g + q) * 128 + pp * 8 + ((j ^ f) << 5));
}
DI unsigned roff_n(int lane, int j) {
  const int g = lane >> 4, q = (lane >> 2) & 3, pp = lane & 3;
  return (unsigned)(g * 512 + q * 64 + pp * 8 + ((j ^ (g & 1)) << 5));
}
DI float colsum_w(lds_t p, float c) {
#pragma unroll
  for (int i = 0; i < 4; ++i) {
    u32x2 v = __builtin_bit_cast(u32x2, __builtin_amdgcn_ds_read_tr16_b64_v4i16((LAS s16x4*)(p + (i >> 1) * 4096 + (i & 1) * 512)));
    c = dot2(v.x, ONES, c); c = dot2(v.y, ONES, c);
  }
  return c;
}
DI float colsum_n(lds_t p, float c) {
#pragma unroll
  for (int i = 0; i < 4; ++i) {
    u32x2 v = __builtin_bit_cast(u32x2, __builtin_amdgcn_ds_read_tr16_b64_v4i16((LAS s16x4*)(p + (i >> 1) * 2048 + (i & 1) * 256)));
    c = dot2(v.x, ONES, c); c = dot2(v.y, ONES, c);
  }
  return c;
}

typedef Cfg<3, 0, 6, 3> C1;
struct P1 {
  static constexpr int NBASE = 2;
  static constexpr bool trans(int n) { return n >= 4; }
  static constexpr bool narrow(int) { return false; }
  static constexpr int base(int i) { return i & 1; }
  static constexpr int imm(int i) { return (i >> 1) * 8192; }
};
struct L1 {
  rsrc_t rA, rQ, rW; unsigned va, vq, vw, aoff, qcol, wcol, so[4], co; int lw;
  float s1, s2, cs;
  DI void issue(lds_t st, int kt) {
    issue_a(rA, st, lw, va, aoff + (unsigned)kt * 128u, 2048u);
    issue_w(rQ, st + 16384, lw, vq, (unsigned)kt * (64u * 6144u) + qcol + 2048u, 6144u);
    issue_w(rQ, st + 16384 + 8192, lw, vq, (unsigned)kt * (64u * 6144u) + qcol + 4096u, 6144u);
    issue_w(rW, st + 16384 + 16384, lw, vw, (unsigned)kt * (64u * 2048u) + wcol, 2048u);
  }
  DI void side(lds_t st) {
#pragma unroll
    for (int i = 0; i < 4; ++i) {
      const u32x4 v = *(const LAS u32x4*)(st + so[i]);
#pragma unroll
      for (int e = 0; e < 4; ++e) { s1 = dot2(v[e], ONES, s1); s2 = dot2(v[e], v[e], s2); }
    }
    cs = colsum_w(st + co, cs);
  }
};
__global__ __launch_bounds__(512, 2) void k1(const u16* __restrict__ x, const u16* __restrict__ wqkv, const u16* __restrict__ w1,
                                             const u16* __restrict__ gw, char* __restrict__ ws) {
  extern __shared__ __attribute__((aligned(16))) char smem_[];
  lds_t smem = (lds_t)smem_;
  typedef C1 C;
  const int w = __builtin_amdgcn_readfirstlane(threadIdx.x >> 6), lane = threadIdx.x & 63;
  const int xcd = blockIdx.x & 7, j = blockIdx.x >> 3;
  const int pm = (xcd & 1) * 8 + (j & 7), pn = (xcd >> 1) * 4 + (j >> 3);
  const int fi = lane & 15, g = lane >> 4;
  const brs_t rW = mk_brs(ws, WS_BYTES);
  LAS f32x2* xst = (LAS f32x2*)(smem + C::XTRA);
  LAS float* xcs = (LAS float*)(smem + C::XTRA + 2048);
  if (w >= 4) {
    const int lw = w - 4;
    L1 L;
    L.rA = mk_rsrc(x, S_ * D_ * 2); L.rQ = mk_rsrc(wqkv, D_ * 3 * D_ * 2); L.rW = mk_rsrc(w1, D_ * D_ * 2);
    L.lw = lw; L.va = voff_a(lane, lw, 2048u); L.vq = voff_w(lane, lw, 6144u); L.vw = voff_w(lane, lw, 2048u);
    L.aoff = (unsigned)pm * 128u * 2048u; L.qcol = (unsigned)pn * 128u; L.wcol = (unsigned)pn * 128u;
    const int row = (lw & 1) * 64 + lane;
#pragma unroll
    for (int i = 0; i < 4; ++i) L.so[i] = (unsigned)(row * 128 + ((((lw >> 1) * 4 + i) ^ ((row >> 1) & 7)) * 16));
    L.co = 16384 + 16384 + roff_w(lane, lw);
    L.s1 = 0.f; L.s2 = 0.f; L.cs = 0.f;
    produce<C>(smem, 16, L);
    xst[(lw >> 1) * 128 + row] = (f32x2){L.s1, L.s2};
    const float cs = sum_g(L.cs);
    if (g == 0) xcs[lw * 16 + fi] = cs;
    wait_lgkm0();
    bar();
    const u32x4 g0 = *(const u32x4*)(gw + lane * 16), g1 = *(const u32x4*)(gw + lane * 16 + 8);
#pragma unroll
    for (int rr = 0; rr < 2; ++rr) {
      const int r = pm * 128 + pn * 8 + lw * 2 + rr;
      const u32x4 x0 = *(const u32x4*)(x + (size_t)r * D_ + lane * 16), x1 = *(const u32x4*)(x + (size_t)r * D_ + lane * 16 + 8);
      float a = 0.f;
#pragma unroll
      for (int e = 0; e < 4; ++e) {
        a = dot2(cvt_pk(bf_lo(x0[e]) * bf_lo(g0[e]), bf_hi(x0[e]) * bf_hi(g0[e])), ONES, a);
        a = dot2(cvt_pk(bf_lo(x1[e]) * bf_lo(g1[e]), bf_hi(x1[e]) * bf_hi(g1[e])), ONES, a);
      }
      a = sum_g(a);
#pragma unroll
      for (int o = 1; o < 16; o <<= 1) a += __shfl_xor(a, o);
      a = rbf(a);
      const float gg = rbf(1.f / (1.f + __expf(-a)));
      if (lane == 0) { u32x2 v; v.x = __builtin_bit_cast(unsigned, gg); v.y = __builtin_bit_cast(unsigned, rbf(1.f - gg)); st8(rW, (unsigned)WS_G + r * 8, v); }
    }
    if (lw == 0 && lane == 0) { u32x2 z = {0u, 0u}; st8(rW, (unsigned)WS_FLAG + blockIdx.x * 128, z); }
    return;
  }
  const int wm = w >> 1, wn = w & 1;
  unsigned boff[2];
  boff[0] = 16384 + roff_w(lane, 2 * wn); boff[1] = 16384 + roff_w(lane, 2 * wn + 1);
  f32x4 acc[4][6];
  consume<C, P1>(smem, 16, acc, wm, lane, boff);
  {
    const int head = pn * 2 + wn, part = pm * 2 + wm;
    f32x4 kv[2][2], z[2];
#pragma unroll
    for (int T = 0; T < 2; ++T) { z[T] = (f32x4){0.f, 0.f, 0.f, 0.f}; kv[T][0] = z[T]; kv[T][1] = z[T]; }
    const u32x4 onesv = {ONES, ONES, ONES, ONES};
#pragma unroll
    for (int mp = 0; mp < 2; ++mp) {
      u32x4 pk[2], vv[2];
#pragma unroll
      for (int T = 0; T < 2; ++T) {
        const f32x4 a = acc[2 * mp][T], b = acc[2 * mp + 1][T];
        pk[T].x = cvt_pk(phi_f(a[0]), phi_f(a[1])); pk[T].y = cvt_pk(phi_f(a[2]), phi_f(a[3]));
        pk[T].z = cvt_pk(phi_f(b[0]), phi_f(b[1])); pk[T].w = cvt_pk(phi_f(b[2]), phi_f(b[3]));
        const f32x4 c = acc[2 * mp][2 + T], d = acc[2 * mp + 1][2 + T];
        vv[T].x = cvt_pk(c[0], c[1]); vv[T].y = cvt_pk(c[2], c[3]); vv[T].z = cvt_pk(d[0], d[1]); vv[T].w = cvt_pk(d[2], d[3]);
      }
#pragma unroll
      for (int T = 0; T < 2; ++T) {
#pragma unroll
        for (int U = 0; U < 2; ++U)
          kv[T][U] = __builtin_amdgcn_mfma_f32_16x16x32_bf16(__builtin_bit_cast(bf16x8, pk[T]), __builtin_bit_cast(bf16x8, vv[U]), kv[T][U], 0, 0, 0);
        z[T] = __builtin_amdgcn_mfma_f32_16x16x32_bf16(__builtin_bit_cast(bf16x8, pk[T]), __builtin_bit_cast(bf16x8, onesv), z[T], 0, 0, 0);
      }
    }
    bar();
    const unsigned kp = (unsigned)WS_KVP + (head * 32 + part) * 4096 + lane * 16;
#pragma unroll
    for (int T = 0; T < 2; ++T)
#pragma unroll
      for (int U = 0; U < 2; ++U) st16f(rW, kp + (T * 2 + U) * 1024, kv[T][U]);
    if (fi == 0) {
#pragma unroll
      for (int T = 0; T < 2; ++T) st16f(rW, (unsigned)WS_ZP + (head * 32 + part) * 128 + T * 64 + g * 16, z[T]);
    }
  }
  {
    const f32x4 cs0 = *(const LAS f32x4*)(xcs + wn * 32 + g * 4), cs1 = *(const LAS f32x4*)(xcs + wn * 32 + 16 + g * 4);
    const int cg = pn >> 2, sh = (cg & 2) ? 2 : 1, dl = (cg & 1) ? -sh : sh;
    const unsigned hs = (unsigned)WS_HS + (pn * 64 + wn * 32 + (g & 1) * 16 + (g & 2) * 4) * 2;
#pragma unroll
    for (int m = 0; m < 4; ++m) {
      const int lr = wm * 64 + m * 16 + fi;
      const f32x2 sa = xst[lr], sb = xst[128 + lr];
      const float mean = (sa.x + sb.x) * (1.f / 1024.f);
      const float var = fmaxf((sa.y + sb.y) * (1.f / 1024.f) - mean * mean, 0.f);
      const float rstd = 1.f / sqrtf(var + 1e-5f), mr = -mean * rstd;
      float h[8];
#pragma unroll
      for (int r = 0; r < 4; ++r) {
        h[r] = gelu_t(__builtin_fmaf(acc[m][4][r], rstd, mr * cs0[r]));
        h[4 + r] = gelu_t(__builtin_fmaf(acc[m][5][r], rstd, mr * cs1[r]));
      }
      const u32x4 v = pair_swap(cvt_pk(h[0], h[1]), cvt_pk(h[2], h[3]), cvt_pk(h[4], h[5]), cvt_pk(h[6], h[7]));
      const int dr = pm * 128 + lr + dl;
      if (dr >= 0 && dr < S_) st16(rW, hs + dr * 2048, v);
    }
    if (dl > 0 ? (pm == 0 && wm == 0) : (pm == 15 && wm == 1)) {
      if (fi < sh) { const int zr = dl > 0 ? fi : S_ - 1 - fi; const u32x4 zz = {0u, 0u, 0u, 0u}; st16(rW, hs + zr * 2048, zz); }
    }
  }
}

typedef Cfg<2, 0, 4, 4> C2;
struct P2 {
  static constexpr int NBASE = 4;
  static constexpr bool trans(int) { return true; }
  static constexpr bool narrow(int) { return false; }
  static constexpr int base(int i) { return i; }
  static constexpr int imm(int) { return 0; }
};
struct L2 {
  rsrc_t rA, rB; unsigned va, vb, aoff, lda2, ldb2, bcol; int lw;
  DI void issue(lds_t st, int kt) {
    issue_a(rA, st, lw, va, aoff + (unsigned)kt * 128u, lda2);
    issue_w(rB, st + 16384, lw, vb, (unsigned)kt * 64u * ldb2 + bcol, ldb2);
    issue_w(rB, st + 16384 + 8192, lw, vb, (unsigned)kt * 64u * ldb2 + bcol + 128u, ldb2);
  }
  DI void side(lds_t) {}
};
__global__ __launch_bounds__(512, 2) void k2(const u16* __restrict__ x, const u16* __restrict__ wqkv, const u16* __restrict__ w2, char* __restrict__ ws) {
  extern __shared__ __attribute__((aligned(16))) char smem_[];
  lds_t smem = (lds_t)smem_;
  typedef C2 C;
  const int w = __builtin_amdgcn_readfirstlane(threadIdx.x >> 6), lane = threadIdx.x & 63;
  const int xcd = blockIdx.x & 7, j = blockIdx.x >> 3;
  const bool isq = xcd < 4;
  const int pm = (xcd & 1) * 8 + (j & 7), pn = ((xcd >> 1) & 1) * 4 + (j >> 3);
  const int fi = lane & 15, g = lane >> 4;
  const brs_t rW = mk_brs(ws, WS_BYTES);
  if (w >= 4) {
    const int lw = w - 4;
    L2 L;
    L.rA = mk_rsrc(isq ? (const void*)x : (const void*)(ws + WS_HS), S_ * D_ * 2);
    L.rB = isq ? mk_rsrc(wqkv, D_ * 3 * D_ * 2) : mk_rsrc(w2, D_ * D_ * 2);
    L.lw = lw; L.lda2 = 2048u; L.ldb2 = isq ? 6144u : 2048u;
    L.va = voff_a(lane, lw, 2048u); L.vb = voff_w(lane, lw, L.ldb2);
    L.aoff = (unsigned)pm * 128u * 2048u; L.bcol = (unsigned)pn * 256u;
    produce<C>(smem, 16, L);
    return;
  }
  const int wm = w >> 1, wn = w & 1;
  const int row0 = pm * 128 + wm * 64 + fi, col0 = pn * 128 + wn * 64 + (g & 1) * 16 + (g & 2) * 4;
  u32x4 xr[4][2];
  if (!isq) {
#pragma unroll
    for (int m = 0; m < 4; ++m)
#pragma unroll
      for (int p = 0; p < 2; ++p) xr[m][p] = *(const u32x4*)(x + (size_t)(row0 + m * 16) * D_ + col0 + p * 32);
  }
  if (w == 0 && blockIdx.x < 160) {
    const int u = blockIdx.x;
    if (u < 128) {
      const int h = u >> 2, T = (u >> 1) & 1, U = u & 1;
      const char* p = ws + WS_KVP + ((size_t)h * 32 * 4 + T * 2 + U) * 1024 + lane * 16;
      f32x4 s = {0.f, 0.f, 0.f, 0.f};
#pragma unroll
      for (int i = 0; i < 32; ++i) s += *(const f32x4*)(p + (size_t)i * 4096);
      u32x2 v; v.x = cvt_pk(s[0], s[1]); v.y = cvt_pk(s[2], s[3]);
      st8(rW, (unsigned)WS_KVF + (h * 2 + U) * 1024 + ((2 * T + (g >> 1)) * 16 + fi) * 16 + (g & 1) * 8, v);
    } else {
      const int h = u - 128;
      const char* p = ws + WS_ZP + (size_t)h * 4096 + lane * 16;
      f32x4 s = {0.f, 0.f, 0.f, 0.f};
#pragma unroll
      for (int i = 0; i < 4; ++i) s += *(const f32x4*)(p + i * 1024);
#pragma unroll
      for (int e = 0; e < 4; ++e) { s[e] = sum_g(s[e]); s[e] += __shfl_xor(s[e], 8); }
      u32x2 v; v.x = cvt_pk(s[0], s[1]); v.y = cvt_pk(s[2], s[3]);
      if (lane < 8) st8(rW, (unsigned)WS_ZF + h * 64 + lane * 8, v);
    }
  }
  unsigned boff[4];
#pragma unroll
  for (int i = 0; i < 4; ++i) boff[i] = 16384 + wn * 8192 + roff_w(lane, i);
  f32x4 acc[4][4];
  consume<C, P2>(smem, 16, acc, wm, lane, boff);
  if (isq) {
#pragma unroll
    for (int m = 0; m < 4; ++m)
#pragma unroll
      for (int p = 0; p < 2; ++p) {
        const f32x4 a = acc[m][2 * p], b = acc[m][2 * p + 1];
        const u32x4 v = pair_swap(cvt_pk(phi_f(a[0]), phi_f(a[1])), cvt_pk(phi_f(a[2]), phi_f(a[3])), cvt_pk(phi_f(b[0]), phi_f(b[1])), cvt_pk(phi_f(b[2]), phi_f(b[3])));
        st16(rW, (unsigned)WS_PQ + ((row0 + m * 16) * D_ + col0 + p * 32) * 2, v);
      }
  } else {
#pragma unroll
    for (int m = 0; m < 4; ++m) {
      float s1 = 0.f, s2 = 0.f;
#pragma unroll
      for (int p = 0; p < 2; ++p) {
        const u32x4 xa = pair_swap(xr[m][p].x, xr[m][p].y, xr[m][p].z, xr[m][p].w);
        const f32x4 a = acc[m][2 * p], b = acc[m][2 * p + 1];
        const unsigned a0 = cvt_pk(a[0] + bf_lo(xa.x), a[1] + bf_hi(xa.x)), a1 = cvt_pk(a[2] + bf_lo(xa.y), a[3] + bf_hi(xa.y));
        const unsigned b0 = cvt_pk(b[0] + bf_lo(xa.z), b[1] + bf_hi(xa.z)), b1 = cvt_pk(b[2] + bf_lo(xa.w), b[3] + bf_hi(xa.w));
        s1 = dot2(a0, ONES, s1); s1 = dot2(a1, ONES, s1); s1 = dot2(b0, ONES, s1); s1 = dot2(b1, ONES, s1);
        s2 = dot2(a0, a0, s2); s2 = dot2(a1, a1, s2); s2 = dot2(b0, b0, s2); s2 = dot2(b1, b1, s2);
        st16(rW, (unsigned)WS_X1 + ((row0 + m * 16) * D_ + col0 + p * 32) * 2, pair_swap(a0, a1, b0, b1));
      }
      s1 = sum_g(s1); s2 = sum_g(s2);
      if (g == 0) { u32x2 v; v.x = __builtin_bit_cast(unsigned, s1); v.y = __builtin_bit_cast(unsigned, s2); st8(rW, (unsigned)WS_ST + (row0 + m * 16) * 128 + (pn * 2 + wn) * 8, v); }
    }
  }
}

typedef Cfg<3, 1, 7, 3> C3;
struct P3 {
  static constexpr int NBASE = 7;
  static constexpr bool trans(int) { return true; }
  static constexpr bool narrow(int i) { return i == 6; }
  static constexpr int base(int i) { return i; }
  static constexpr int imm(int) { return 0; }
};
struct L3 {
  rsrc_t rA, rB; unsigned va, vb, vn, aoff, wcol, ncol, cw, cn; int lw;
  float cs[4];
  DI void issue(lds_t st, int kt) {
    issue_a(rA, st, lw, va, aoff + (unsigned)kt * 128u, 2048u);
    const unsigned kb = (unsigned)kt * (64u * 7168u);
#pragma unroll
    for (int b = 0; b < 3; ++b) issue_w(rB, st + 16384 + b * 8192, lw, vb, kb + wcol + b * 128u, 7168u);
    glds16(rB, st + 16384 + 24576 + lw * 1024, vn, kb + ncol + (unsigned)lw * (16u * 7168u));
  }
  DI void side(lds_t st) {
#pragma unroll
    for (int b = 0; b < 3; ++b) cs[b] = colsum_w(st + cw + b * 8192, cs[b]);
    cs[3] = colsum_n(st + cn, cs[3]);
  }
};
__global__ __launch_bounds__(512, 2) void k3(const u16* __restrict__ w3, char* __restrict__ ws) {
  extern __shared__ __attribute__((aligned(16))) char smem_[];
  lds_t smem = (lds_t)smem_;
  typedef C3 C;
  const int w = __builtin_amdgcn_readfirstlane(threadIdx.x >> 6), lane = threadIdx.x & 63;
  const int xcd = blockIdx.x & 7, j = blockIdx.x >> 3;
  const int pm = (xcd & 1) * 8 + (j & 7), pn = (xcd >> 1) * 4 + (j >> 3);
  const int fi = lane & 15, g = lane >> 4;
  const brs_t rW = mk_brs(ws, WS_BYTES);
  const int wc0 = (pn & 1) ? 32 : 0, nc0 = (pn & 1) ? 0 : 192;
  LAS f32x4* rst = (LAS f32x4*)(smem + C::XTRA);
  LAS float* xcs = (LAS float*)(smem + C::XTRA + 2048);
  if (w >= 4) {
    const int lw = w - 4;
    L3 L;
    L.rA = mk_rsrc(ws + WS_X1, S_ * D_ * 2); L.rB = mk_rsrc(w3, D_ * F_ * 2);
    L.lw = lw; L.va = voff_a(lane, lw, 2048u); L.vb = voff_w(lane, lw, 7168u); L.vn = voff_n(lane, 7168u);
    L.aoff = (unsigned)pm * 128u * 2048u; L.wcol = (unsigned)(pn * 224 + wc0) * 2u; L.ncol = (unsigned)(pn * 224 + nc0) * 2u;
    L.cw = 16384 + roff_w(lane, lw); L.cn = 16384 + 24576 + roff_n(lane, lw & 1);
#pragma unroll
    for (int b = 0; b < 4; ++b) L.cs[b] = 0.f;
    produce<C>(smem, 16, L);
#pragma unroll
    for (int b = 0; b < 4; ++b) {
      const float cs = sum_g(L.cs[b]);
      if (g == 0 && (b < 3 || lw < 2)) xcs[b * 64 + lw * 16 + fi] = cs;
    }
    wait_lgkm0();
    bar();
    const int head = pn * 2 + (lw & 1), r0 = pm * 128 + (lw >> 1) * 64 + fi;
    const bf16x8 kf0 = *(const bf16x8*)(ws + WS_KVF + (size_t)(head * 2) * 1024 + lane * 16);
    const bf16x8 kf1 = *(const bf16x8*)(ws + WS_KVF + (size_t)(head * 2 + 1) * 1024 + lane * 16);
    const bf16x8 zf = *(const bf16x8*)(ws + WS_ZF + (size_t)head * 64 + g * 16);
    bf16x8 pq[4]; float gg[4];
#pragma unroll
    for (int m = 0; m < 4; ++m) {
      pq[m] = *(const bf16x8*)((const u16*)(ws + WS_PQ) + (size_t)(r0 + m * 16) * D_ + head * 32 + g * 8);
      gg[m] = *(const float*)(ws + WS_G + (size_t)(r0 + m * 16) * 8);
    }
#pragma unroll
    for (int m = 0; m < 4; ++m) {
      const f32x4 zero = {0.f, 0.f, 0.f, 0.f};
      const f32x4 n0 = __builtin_amdgcn_mfma_f32_16x16x32_bf16(kf0, pq[m], zero, 0, 0, 0);
      const f32x4 n1 = __builtin_amdgcn_mfma_f32_16x16x32_bf16(kf1, pq[m], zero, 0, 0, 0);
      const f32x4 dn = __builtin_amdgcn_mfma_f32_16x16x32_bf16(zf, pq[m], zero, 0, 0, 0);
      const float sc = gg[m] / (dn[0] + 1e-6f);
      const u32x4 v = pair_swap(cvt_pk(n0[0] * sc, n0[1] * sc), cvt_pk(n0[2] * sc, n0[3] * sc), cvt_pk(n1[0] * sc, n1[1] * sc), cvt_pk(n1[2] * sc, n1[3] * sc));
      st16(rW, (unsigned)WS_AU + ((r0 + m * 16) * LDAU + head * 32 + (g & 1) * 16 + (g & 2) * 4) * 2, v);
    }
    return;
  }
  const int wm = w >> 1, wn = w & 1;
  {
    const int lr = w * 32 + (lane >> 1), r = pm * 128 + lr;
    const char* p = ws + WS_ST + (size_t)r * 128 + (lane & 1) * 64;
    f32x4 s = {0.f, 0.f, 0.f, 0.f};
#pragma unroll
    for (int i = 0; i < 4; ++i) s += *(const f32x4*)(p + i * 16);
    const float omg = *(const float*)(ws + WS_G + (size_t)r * 8 + 4);
    float s1 = s[0] + s[2], s2 = s[1] + s[3];
    s1 += __shfl_xor(s1, 1); s2 += __shfl_xor(s2, 1);
    const float mean = s1 * (1.f / 1024.f);
    const float var = fmaxf(s2 * (1.f / 1024.f) - mean * mean, 0.f);
    const float rstd = 1.f / sqrtf(var + 1e-5f);
    if (!(lane & 1)) rst[lr] = (f32x4){rstd, -mean * rstd, omg, 0.f};
  }
  unsigned boff[7];
#pragma unroll
  for (int i = 0; i < 4; ++i) boff[i] = 16384 + wn * 8192 + roff_w(lane, i);
  boff[4] = 16384 + 16384 + roff_w(lane, 2 * wn); boff[5] = 16384 + 16384 + roff_w(lane, 2 * wn + 1);
  boff[6] = 16384 + 24576 + roff_n(lane, wn);
  f32x4 acc[4][7];
  consume<C, P3>(smem, 16, acc, wm, lane, boff);
  bar();
  f32x4 cs[7];
#pragma unroll
  for (int i = 0; i < 4; ++i) cs[i] = *(const LAS f32x4*)(xcs + wn * 64 + i * 16 + g * 4);
  cs[4] = *(const LAS f32x4*)(xcs + 128 + wn * 32 + g * 4); cs[5] = *(const LAS f32x4*)(xcs + 128 + wn * 32 + 16 + g * 4);
  cs[6] = *(const LAS f32x4*)(xcs + 192 + wn * 16 + g * 4);
  const int sw = (g & 1) * 16 + (g & 2) * 4;
#pragma unroll
  for (int m = 0; m < 4; ++m) {
    const int lr = wm * 64 + m * 16 + fi;
    const f32x4 rs = rst[lr];
    const unsigned rowp = (unsigned)WS_AU + ((pm * 128 + lr) * LDAU + 1024 + pn * 224) * 2;
    unsigned pk[7][2];
#pragma unroll
    for (int n = 0; n < 7; ++n) {
      float h[4];
#pragma unroll
      for (int r = 0; r < 4; ++r) h[r] = rs[2] * gelu_t(__builtin_fmaf(acc[m][n][r], rs[0], rs[1] * cs[n][r]));
      pk[n][0] = cvt_pk(h[0], h[1]); pk[n][1] = cvt_pk(h[2], h[3]);
    }
    st16(rW, rowp + (wc0 + wn * 64 + sw) * 2, pair_swap(pk[0][0], pk[0][1], pk[1][0], pk[1][1]));
    st16(rW, rowp + (wc0 + wn * 64 + 32 + sw) * 2, pair_swap(pk[2][0], pk[2][1], pk[3][0], pk[3][1]));
    st16(rW, rowp + (wc0 + 128 + wn * 32 + sw) * 2, pair_swap(pk[4][0], pk[4][1], pk[5][0], pk[5][1]));
    u32x2 v; v.x = pk[6][0]; v.y = pk[6][1];
    st8(rW, rowp + (nc0 + wn * 16 + g * 4) * 2, v);
  }
}

typedef Cfg<2, 0, 4, 4> C4;
struct L4 {
  rsrc_t rA, rO, rW; unsigned va, vb, aoff, bcol; int lw, kt0;
  DI void issue(lds_t st, int t) {
    const int kt = kt0 + t;
    issue_a(rA, st, lw, va, aoff + (unsigned)kt * 128u, LDAU * 2u);
    const bool o = kt < 16;
    rsrc_t rB; rB.x = o ? rO.x : rW.x; rB.y = o ? rO.y : rW.y; rB.z = o ? rO.z : rW.z; rB.w = rO.w;
    const unsigned kb = (unsigned)(o ? kt : kt - 16) * (64u * 2048u) + bcol;
    issue_w(rB, st + 16384, lw, vb, kb, 2048u);
    issue_w(rB, st + 16384 + 8192, lw, vb, kb + 128u, 2048u);
  }
  DI void side(lds_t) {}
};
__global__ __launch_bounds__(512, 2) void k4(const u16* __restrict__ wo, const u16* __restrict__ w4, char* __restrict__ ws, u16* __restrict__ out) {
  extern __shared__ __attribute__((aligned(16))) char smem_[];
  lds_t smem = (lds_t)smem_;
  typedef C4 C;
  const int w = __builtin_amdgcn_readfirstlane(threadIdx.x >> 6), lane = threadIdx.x & 63;
  const int xcd = blockIdx.x & 7, j = blockIdx.x >> 3;
  const int hf = xcd >> 2;
  const int pm = (xcd & 1) * 8 + (j & 7), pn = ((xcd >> 1) & 1) * 4 + (j >> 3);
  const int fi = lane & 15, g = lane >> 4;
  const brs_t rW = mk_brs(ws, WS_BYTES);
  if (w >= 4) {
    const int lw = w - 4;
    L4 L;
    L.rA = mk_rsrc(ws + WS_AU, S_ * LDAU * 2); L.rO = mk_rsrc(wo, D_ * D_ * 2); L.rW = mk_rsrc(w4, F_ * D_ * 2);
    L.lw = lw; L.kt0 = hf * 36; L.va = voff_a(lane, lw, LDAU * 2u); L.vb = voff_w(lane, lw, 2048u);
    L.aoff = (unsigned)pm * 128u * (LDAU * 2u); L.bcol = (unsigned)pn * 256u;
    produce<C>(smem, 36, L);
    return;
  }
  const int wm = w >> 1, wn = w & 1;
  const int row0 = pm * 128 + wm * 64 + hf * 32 + fi, col0 = pn * 128 + wn * 64 + (g & 1) * 16 + (g & 2) * 4;
  u32x4 xr[2][2]; float omg[2];
#pragma unroll
  for (int m = 0; m < 2; ++m) {
#pragma unroll
    for (int p = 0; p < 2; ++p) xr[m][p] = *(const u32x4*)((const u16*)(ws + WS_X1) + (size_t)(row0 + m * 16) * D_ + col0 + p * 32);
    omg[m] = *(const float*)(ws + WS_G + (size_t)(row0 + m * 16) * 8 + 4);
  }
  unsigned boff[4];
#pragma unroll
  for (int i = 0; i < 4; ++i) boff[i] = 16384 + wn * 8192 + roff_w(lane, i);
  f32x4 acc[4][4];
  consume<C, P2>(smem, 36, acc, wm, lane, boff);
  f32x4 own[2][4];
  {
    const unsigned ob = (unsigned)WS_XCH + (blockIdx.x * 4 + w) * 8192 + lane * 16;
#pragma unroll
    for (int m = 0; m < 2; ++m)
#pragma unroll
      for (int n = 0; n < 4; ++n) {
        f32x4 snd;
#pragma unroll
        for (int r = 0; r < 4; ++r) { snd[r] = hf ? acc[m][n][r] : acc[2 + m][n][r]; own[m][n][r] = hf ? acc[2 + m][n][r] : acc[m][n][r]; }
        st16f(rW, ob + (m * 4 + n) * 1024, snd);
      }
    wait_vm<0>();
    bar();
    if (threadIdx.x == 0) __builtin_amdgcn_raw_buffer_store_b32(1u, rW, (int)((unsigned)WS_FLAG + blockIdx.x * 128), 0, 16);
  }
  {
    const char* fp = ws + WS_FLAG + (size_t)(blockIdx.x ^ 4) * 128;
    unsigned v = 0;
    for (int it = 0; it < (1 << 22); ++it) {
      asm volatile("global_load_dword %0, %1, off sc1\n\ts_waitcnt vmcnt(0)" : "=v"(v) : "v"(fp) : "memory");
      if (__builtin_amdgcn_readfirstlane(v) != 0u) break;
      __builtin_amdgcn_s_sleep(1);
    }
    const unsigned ib = (unsigned)WS_XCH + ((blockIdx.x ^ 4) * 4 + w) * 8192 + lane * 16;
    f32x4 o[2][4];
#pragma unroll
    for (int m = 0; m < 2; ++m)
#pragma unroll
      for (int n = 0; n < 4; ++n) o[m][n] = __builtin_bit_cast(f32x4, __builtin_amdgcn_raw_buffer_load_b128(rW, (int)(ib + (m * 4 + n) * 1024), 0, 16));
#pragma unroll
    for (int m = 0; m < 2; ++m)
#pragma unroll
      for (int p = 0; p < 2; ++p) {
        const u32x4 xa = pair_swap(xr[m][p].x, xr[m][p].y, xr[m][p].z, xr[m][p].w);
        const f32x4 a = own[m][2 * p] + o[m][2 * p], b = own[m][2 * p + 1] + o[m][2 * p + 1];
        const float q = omg[m];
        const unsigned a0 = cvt_pk(__builtin_fmaf(q, bf_lo(xa.x), a[0]), __builtin_fmaf(q, bf_hi(xa.x), a[1]));
        const unsigned a1 = cvt_pk(__builtin_fmaf(q, bf_lo(xa.y), a[2]), __builtin_fmaf(q, bf_hi(xa.y), a[3]));
        const unsigned b0 = cvt_pk(__builtin_fmaf(q, bf_lo(xa.z), b[0]), __builtin_fmaf(q, bf_hi(xa.z), b[1]));
        const unsigned b1 = cvt_pk(__builtin_fmaf(q, bf_lo(xa.w), b[2]), __builtin_fmaf(q, bf_hi(xa.w), b[3]));
        *(u32x4*)(out + (size_t)(row0 + m * 16) * D_ + col0 + p * 32) = pair_swap(a0, a1, b0, b1);
      }
  }
}

extern "C" void kernel_launch(void* const* d_in, const int* in_sizes, int n_in, void* d_out, int out_size, void* d_ws, size_t ws_size,
                              hipStream_t stream) {
  const u16* x = (const u16*)d_in[0];
  const u16* wqkv = (const u16*)d_in[1];
  const u16* wo = (const u16*)d_in[2];
  const u16* w1 = (const u16*)d_in[4];
  const u16* w2 = (const u16*)d_in[5];
  const u16* w3 = (const u16*)d_in[6];
  const u16* w4 = (const u16*)d_in[7];
  const u16* gw = (const u16*)d_in[8];
  char* ws = (char*)d_ws;
  (void)hipFuncSetAttribute((const void*)k1, hipFuncAttributeMaxDynamicSharedMemorySize, C1::LDS);
  (void)hipFuncSetAttribute((const void*)k2, hipFuncAttributeMaxDynamicSharedMemorySize, C2::LDS);
  (void)hipFuncSetAttribute((const void*)k3, hipFuncAttributeMaxDynamicSharedMemorySize, C3::LDS);
  (void)hipFuncSetAttribute((const void*)k4, hipFuncAttributeMaxDynamicSharedMemorySize, C4::LDS);
  k1<<<256, 512, C1::LDS, stream>>>(x, wqkv, w1, gw, ws);
  k2<<<256, 512, C2::LDS, stream>>>(x, wqkv, w2, ws);
  k3<<<256, 512, C3::LDS, stream>>>(w3, ws);
  k4<<<256, 512, C4::LDS, stream>>>(wo, w4, ws, (u16*)d_out);
}
```

```cpp
#include <hip/hip_runtime.h>

typedef unsigned short u16;
typedef unsigned long long u64;
typedef short bf16x8 __attribute__((ext_vector_type(8)));
typedef short s16x4 __attribute__((ext_vector_type(4)));
typedef float f32x4 __attribute__((ext_vector_type(4)));
typedef float f32x2 __attribute__((ext_vector_type(2)));
typedef unsigned u32x4 __attribute__((ext_vector_type(4)));
typedef unsigned u32x2 __attribute__((ext_vector_type(2)));
typedef __bf16 bf16x2_t __attribute__((ext_vector_type(2)));
#define LAS __attribute__((address_space(3)))
typedef LAS char* lds_t;
typedef u32x4 rsrc_t;
#define DI __device__ __forceinline__

constexpr int S_ = 2048, D_ = 1024, F_ = 3584;
constexpr int LDAU = 4608 + 64;
constexpr size_t MB = 1u << 20;
constexpr size_t WS_HS = 0, WS_PQ = 4 * MB, WS_X1 = 8 * MB, WS_AU = 12 * MB, WS_KVP = 32 * MB, WS_ZP = 36 * MB,
                 WS_KVF = 36 * MB + 512 * 1024, WS_ZF = 36 * MB + 768 * 1024, WS_ST = 37 * MB, WS_G = 37 * MB + 512 * 1024,
                 WS_RS1 = 37 * MB + 640 * 1024, WS_CS1 = 37 * MB + 768 * 1024, WS_FLAG = 38 * MB, WS_CS3 = 39 * MB, WS_XCH = 40 * MB;

DI rsrc_t mk_rsrc(const void* p, unsigned bytes) {
  u64 a = (u64)p; rsrc_t r; r.x = (unsigned)a; r.y = (unsigned)(a >> 32) & 0xffffu; r.z = bytes; r.w = 0x00020000u; return r;
}
DI void glds16(rsrc_t r, lds_t dst, unsigned voff, unsigned soff) {
  asm volatile("s_mov_b32 m0, %0\n\ts_nop 0\n\tbuffer_load_dwordx4 %1, %2, %3 offen lds" ::"s"((unsigned)(size_t)dst), "v"(voff), "s"(r), "s"(soff) : "memory");
}
DI void gtouch(rsrc_t r, lds_t dst, unsigned voff, unsigned soff) {
  asm volatile("s_mov_b32 m0, %0\n\ts_nop 0\n\tbuffer_load_dword %1, %2, %3 offen lds" ::"s"((unsigned)(size_t)dst), "v"(voff), "s"(r), "s"(soff) : "memory");
}
template <int N> DI void wait_vm() { asm volatile("s_waitcnt vmcnt(%0)" ::"n"(N) : "memory"); }
DI void wait_lgkm0() { asm volatile("" ::: "memory"); __builtin_amdgcn_s_waitcnt(0xC07F); asm volatile("" ::: "memory"); }
DI void bar() { asm volatile("" ::: "memory"); __builtin_amdgcn_s_barrier(); asm volatile("" ::: "memory"); }
DI unsigned cvt_pk(float lo, float hi) { f32x2 v = {lo, hi}; bf16x2_t r = __builtin_convertvector(v, bf16x2_t); return __builtin_bit_cast(unsigned, r); }
DI float bf_lo(unsigned u) { return __builtin_bit_cast(float, u << 16); }
DI float bf_hi(unsigned u) { return __builtin_bit_cast(float, u & 0xffff0000u); }
DI float rbf(float f) { return bf_lo(cvt_pk(f, 0.f) & 0xffffu); }
DI float dot2(unsigned a, unsigned b, float c) { return __builtin_amdgcn_fdot2_f32_bf16(__builtin_bit_cast(bf16x2_t, a), __builtin_bit_cast(bf16x2_t, b), c, false); }
constexpr unsigned ONES = 0x3f803f80u;
typedef __amdgpu_buffer_rsrc_t brs_t;
constexpr unsigned WS_BYTES = 48u << 20;
DI brs_t mk_brs(void* p, unsigned bytes) { return __builtin_amdgcn_make_buffer_rsrc(p, (short)0, (int)bytes, 0x00020000); }
#define SAUX 16
DI void st16(brs_t r, unsigned off, u32x4 v) { __builtin_amdgcn_raw_buffer_store_b128(v, r, (int)off, 0, SAUX); }
DI void st8(brs_t r, unsigned off, u32x2 v) { __builtin_amdgcn_raw_buffer_store_b64(v, r, (int)off, 0, SAUX); }
DI void st16f(brs_t r, unsigned off, f32x4 v) { __builtin_amdgcn_raw_buffer_store_b128(__builtin_bit_cast(u32x4, v), r, (int)off, 0, 16); }
#define STAMP(kid, i) do {} while (0)
#define STAMP_END(kid, i) do {} while (0)
#define STAMP0() do {} while (0)
DI float sum_g(float v) {
  unsigned u = __builtin_bit_cast(unsigned, v);
  auto a = __builtin_amdgcn_permlane16_swap(u, u, false, false);
  const unsigned a0 = a[0], a1 = a[1];
  v = __uint_as_float(a0) + __uint_as_float(a1);
  u = __builtin_bit_cast(unsigned, v);
  auto b = __builtin_amdgcn_permlane32_swap(u, u, false, false);
  const unsigned b0 = b[0], b1 = b[1];
  return __uint_as_float(b0) + __uint_as_float(b1);
}
DI float gelu_t(float v) {
  const float e = __builtin_amdgcn_exp2f(v * __builtin_fmaf(v * v, -0.10294323970f, -2.30220819814f));
  return v * __builtin_amdgcn_rcpf(1.f + e);
}
DI float phi_f(float t) { return (t > 0.f ? t + 1.f : __builtin_amdgcn_exp2f(t * 1.44269504089f)) + 1e-6f; }
DI u32x4 pair_swap(unsigned a0, unsigned a1, unsigned b0, unsigned b1) {
  auto r0 = __builtin_amdgcn_permlane16_swap(a0, b0, false, false);
  auto r1 = __builtin_amdgcn_permlane16_swap(a1, b1, false, false);
  u32x4 v; v.x = r0[0]; v.y = r1[0]; v.z = r0[1]; v.w = r1[1]; return v;
}
DI int quad_col(int lane) { const int g = lane >> 4; return ((lane >> 3) & 1) * 32 + (g & 1) * 16 + (g & 2) * 4; }
DI void row_xchg(u32x4& a, u32x4& b) {
#pragma unroll
  for (int e = 0; e < 4; ++e) {
    const unsigned x = a[e], y = b[e];
    a[e] = (unsigned)__builtin_amdgcn_update_dpp((int)x, (int)y, 0x128, 0xf, 0xc, false);
    b[e] = (unsigned)__builtin_amdgcn_update_dpp((int)y, (int)x, 0x128, 0xf, 0x3, false);
  }
}
DI void store_quad(brs_t r, unsigned off  , unsigned pitch8  , const unsigned (&p)[4][2]) {
  u32x4 a = pair_swap(p[0][0], p[0][1], p[1][0], p[1][1]), b = pair_swap(p[2][0], p[2][1], p[3][0], p[3][1]);
  row_xchg(a, b);
  st16(r, off, a); st16(r, off + pitch8, b);
}
DI void unpack_quad(u32x4 a, u32x4 b, unsigned (&p)[4][2]) {
  row_xchg(a, b);
  const u32x4 x = pair_swap(a.x, a.y, a.z, a.w), y = pair_swap(b.x, b.y, b.z, b.w);
  p[0][0] = x.x; p[0][1] = x.y; p[1][0] = x.z; p[1][1] = x.w; p[2][0] = y.x; p[2][1] = y.y; p[3][0] = y.z; p[3][1] = y.w;
}
DI float sum_r8(float v) {
  v += __int_as_float(__builtin_amdgcn_update_dpp(0, __float_as_int(v), 0x128, 0xf, 0xf, false));
  return sum_g(v);
}

template <int NWB_, int NNB_, int NT_, int NS_>
struct Cfg {
  static constexpr int NWB = NWB_, NNB = NNB_, NT = NT_, NS = NS_, MT = 4;
  static constexpr int A_BYTES = 16384, STAGE = A_BYTES + NWB * 8192 + NNB * 4096, XTRA = NS * STAGE, LDS = XTRA + 8192;
  static constexpr int L = 4 + 2 * NWB + NNB;
};
template <class C> struct Frags { bf16x8 a[C::MT]; bf16x8 b[C::NT]; };

DI bf16x8 rd_bfrag(lds_t p, int hi_off) {
  s16x4 lo = __builtin_amdgcn_ds_read_tr16_b64_v4i16((LAS s16x4*)p);
  s16x4 hi = __builtin_amdgcn_ds_read_tr16_b64_v4i16((LAS s16x4*)(p + hi_off));
  return __builtin_shufflevector(lo, hi, 0, 1, 2, 3, 4, 5, 6, 7);
}
template <class C, class P>
DI void read_frags(Frags<C>& F, lds_t st, int ks, unsigned aoff, const unsigned (&boff)[P::NBASE]) {
#pragma unroll
  for (int i = 0; i < C::NT; ++i)
    F.b[i] = P::narrow(i) ? rd_bfrag(st + boff[P::base(i)] + ks * 2048, 256) : rd_bfrag(st + boff[P::base(i)] + P::imm(i) + ks * 4096, 512);
#pragma unroll
  for (int m = 0; m < C::MT; ++m) F.a[m] = *(const LAS bf16x8*)(st + aoff + m * 2048);
}
template <class C, class P>
DI void mma(f32x4 (&acc)[C::MT][C::NT], const Frags<C>& F) {
#pragma unroll
  for (int n = 0; n < C::NT; ++n)
#pragma unroll
    for (int m = 0; m < C::MT; ++m)
      acc[m][n] = P::trans(n) ? __builtin_amdgcn_mfma_f32_16x16x32_bf16(F.b[n], F.a[m], acc[m][n], 0, 0, 0)
                              : __builtin_amdgcn_mfma_f32_16x16x32_bf16(F.a[m], F.b[n], acc[m][n], 0, 0, 0);
}
template <class C>
DI void sched_hint() {
#pragma unroll
  for (int i = 0; i < C::MT * C::NT; ++i) {
    __builtin_amdgcn_sched_group_barrier(0x8, 1, 0);
    if (i < C::MT + 2 * C::NT) __builtin_amdgcn_sched_group_barrier(0x100, 1, 0);
  }
  __builtin_amdgcn_sched_barrier(0);
}
template <class C, class P>
DI void consume(lds_t smem, int nt, f32x4 (&acc)[C::MT][C::NT], int wm, int lane, const unsigned (&boff)[P::NBASE]) {
  const int fi = lane & 15, g = lane >> 4, f = (fi >> 1) & 7;
  const unsigned aoff0 = (unsigned)((wm * 64 + fi) * 128 + ((g ^ f) * 16));
  const unsigned aoff1 = (unsigned)((wm * 64 + fi) * 128 + (((4 + g) ^ f) * 16));
#pragma unroll
  for (int m = 0; m < C::MT; ++m)
#pragma unroll
    for (int n = 0; n < C::NT; ++n) acc[m][n] = (f32x4){0.f, 0.f, 0.f, 0.f};
  Frags<C> F0, F1;
  bar();
  read_frags<C, P>(F0, smem, 0, aoff0, boff);
  int rs = 0;
  for (int t = 0; t < nt - 1; ++t) {
    wait_lgkm0();
    __builtin_amdgcn_sched_barrier(0);
    read_frags<C, P>(F1, smem + rs * C::STAGE, 1, aoff1, boff);
    mma<C, P>(acc, F0);
    sched_hint<C>();
    rs = (rs + 1 == C::NS) ? 0 : rs + 1;
    wait_lgkm0();
    bar();
    __builtin_amdgcn_sched_barrier(0);
    read_frags<C, P>(F0, smem + rs * C::STAGE, 0, aoff0, boff);
    mma<C, P>(acc, F1);
    sched_hint<C>();
  }
  wait_lgkm0();
  __builtin_amdgcn_sched_barrier(0);
  read_frags<C, P>(F1, smem + rs * C::STAGE, 1, aoff1, boff);
  mma<C, P>(acc, F0);
  sched_hint<C>();
  mma<C, P>(acc, F1);
}
template <class C, class LD>
DI void produce(lds_t smem, int nt, LD& L) {
#pragma unroll
  for (int s = 0; s < C::NS - 1; ++s) L.issue(smem + s * C::STAGE, s);
  wait_vm<(C::NS - 2) * C::L>();
  bar();
  int ws = C::NS - 1;
  for (int t = 0; t < nt - C::NS + 1; ++t) {
    L.issue(smem + ws * C::STAGE, t + C::NS - 1);
    wait_vm<(C::NS - 2) * C::L>();
    ws = (ws + 1 == C::NS) ? 0 : ws + 1;
    bar();
  }
  L.tail(smem + ws * C::STAGE);
  if (C::NS == 4) { wait_vm<C::L + LD::NTAIL>(); bar(); }
  wait_vm<LD::NTAIL>();
  bar();
}
DI unsigned voff_a(int lane, int lw, unsigned lda2) { return (unsigned)(lane >> 3) * lda2 + (unsigned)(((lane & 7) ^ (((lw & 1) * 4 + (lane >> 4)) & 7)) * 16); }
DI unsigned voff_w(int lane, int lw, unsigned ldb2) { return (unsigned)(lane >> 3) * ldb2 + (unsigned)(((lane & 7) ^ (((lane >> 4) & 1) * 2 + (lw & 1) * 4)) * 16); }
DI unsigned voff_n(int lane, unsigned ldb2) { return (unsigned)(lane >> 2) * ldb2 + (unsigned)(((lane & 3) ^ (((lane >> 5) & 1) * 2)) * 16); }
DI void issue_a(rsrc_t rA, lds_t st, int lw, unsigned voff, unsigned soff  , unsigned lda2) {
#pragma unroll
  for (int i = 0; i < 4; ++i) glds16(rA, st + (lw + 4 * i) * 1024, voff, soff + (unsigned)(lw + 4 * i) * 8u * lda2);
}
DI void issue_w(rsrc_t rB, lds_t blk, int lw, unsigned voff, unsigned soff  , unsigned ldb2) {
#pragma unroll
  for (int i = 0; i < 2; ++i) glds16(rB, blk + (lw + 4 * i) * 1024, voff, soff + (unsigned)(lw + 4 * i) * 8u * ldb2);
}
DI unsigned roff_w(int lane, int j) {
  const int g = lane >> 4, q = (lane >> 2) & 3, pp = lane & 3, f = ((q >> 1) & 1) | ((g & 1) << 1);
  return (unsigned)((8 * g + q) * 128 + pp * 8 + ((j ^ f) << 5));
}
DI unsigned roff_n(int lane, int j) {
  const int g = lane >> 4, q = (lane >> 2) & 3, pp = lane & 3;
  return (unsigned)(g * 512 + q * 64 + pp * 8 + ((j ^ (g & 1)) << 5));
}

__global__ __launch_bounds__(512) void k0(const u16* __restrict__ x, const u16* __restrict__ wqkv, const u16* __restrict__ w1,
                                          const u16* __restrict__ gw, char* __restrict__ ws) {
  __shared__ float red[8][64];
  STAMP0();
  const int w = threadIdx.x >> 6, lane = threadIdx.x & 63, b = blockIdx.x;
  const brs_t rW = mk_brs(ws, WS_BYTES);
  const int r = b * 8 + w;
  const u32x4 x0 = *(const u32x4*)(x + (size_t)r * D_ + lane * 8), x1 = *(const u32x4*)(x + (size_t)r * D_ + 512 + lane * 8);
  const u32x4 wv = *(const u32x4*)(w1 + (size_t)((b >> 4) * 64 + w * 8 + (lane >> 3)) * D_ + (b & 15) * 64 + (lane & 7) * 8);
  const u32x4 g0 = *(const u32x4*)(gw + lane * 8), g1 = *(const u32x4*)(gw + 512 + lane * 8);
  unsigned touch = 0;
  if (lane < 16) touch = *(const unsigned*)(wqkv + (size_t)(b * 4 + (w >> 1)) * 3072 + 1024 + ((w & 1) * 16 + lane) * 64);
  float s = 0.f, a = 0.f;
#pragma unroll
  for (int e = 0; e < 4; ++e) {
    s = dot2(x0[e], ONES, s); s = dot2(x1[e], ONES, s);
    a = dot2(cvt_pk(bf_lo(x0[e]) * bf_lo(g0[e]), bf_hi(x0[e]) * bf_hi(g0[e])), ONES, a);
    a = dot2(cvt_pk(bf_lo(x1[e]) * bf_lo(g1[e]), bf_hi(x1[e]) * bf_hi(g1[e])), ONES, a);
  }
  s = sum_r8(s); a = sum_r8(a);
#pragma unroll
  for (int o = 1; o < 8; o <<= 1) { s += __shfl_xor(s, o); a += __shfl_xor(a, o); }
  const float mean = s * (1.f / 1024.f);
  float q = 0.f;
#pragma unroll
  for (int e = 0; e < 4; ++e) {
    float d;
    d = bf_lo(x0[e]) - mean; q = __builtin_fmaf(d, d, q); d = bf_hi(x0[e]) - mean; q = __builtin_fmaf(d, d, q);
    d = bf_lo(x1[e]) - mean; q = __builtin_fmaf(d, d, q); d = bf_hi(x1[e]) - mean; q = __builtin_fmaf(d, d, q);
  }
  q = sum_r8(q);
#pragma unroll
  for (int o = 1; o < 8; o <<= 1) q += __shfl_xor(q, o);
  const float rstd = 1.f / sqrtf(q * (1.f / 1024.f) + 1e-5f);
  const float gg = rbf(1.f / (1.f + __expf(-rbf(a))));
  if (lane == 0) {
    u32x2 v; v.x = __float_as_uint(gg); v.y = __float_as_uint(rbf(1.f - gg)); st8(rW, (unsigned)WS_G + r * 8, v);
    v.x = __float_as_uint(rstd); v.y = __float_as_uint(-mean * rstd); st8(rW, (unsigned)WS_RS1 + r * 8, v);
  }
#pragma unroll
  for (int e = 0; e < 4; ++e) {
    const float lo = sum_r8(bf_lo(wv[e])), hi = sum_r8(bf_hi(wv[e]));
    if (lane < 8) { red[w][lane * 8 + e * 2] = lo; red[w][lane * 8 + e * 2 + 1] = hi; }
  }
  __syncthreads();
  if (w == 0) {
    float c = 0.f;
#pragma unroll
    for (int i = 0; i < 8; ++i) c += red[i][lane];
    __builtin_amdgcn_raw_buffer_store_b32(__float_as_uint(c), rW, (int)((unsigned)WS_CS1 + ((b >> 4) * 1024 + (b & 15) * 64 + lane) * 4), 0, 16);
    if (lane == 0) { u32x2 z = {0u, 0u}; st8(rW, (unsigned)WS_FLAG + b * 128, z); }
  }
  asm volatile("" ::"v"(touch));
  STAMP_END(4, 2);
}

typedef Cfg<3, 0, 6, 3> C1;
struct P1 {
  static constexpr int NBASE = 2;
  static constexpr bool trans(int n) { return n >= 4; }
  static constexpr bool narrow(int) { return false; }
  static constexpr int base(int i) { return i & 1; }
  static constexpr int imm(int i) { return (i >> 1) * 8192; }
};
struct L1 {
  static constexpr int NTAIL = 3;
  rsrc_t rA, rQ, rW, rS, rN; unsigned va, vq, vw, aoff, qcol, wcol; int lw, lane, pm, pn; lds_t xtra;
  DI void issue(lds_t st, int kt) {
    issue_a(rA, st, lw, va, aoff + (unsigned)kt * 128u, 2048u);
    issue_w(rQ, st + 16384, lw, vq, (unsigned)kt * (64u * 6144u) + qcol + 2048u, 6144u);
    issue_w(rQ, st + 16384 + 8192, lw, vq, (unsigned)kt * (64u * 6144u) + qcol + 4096u, 6144u);
    issue_w(rW, st + 16384 + 16384, lw, vw, (unsigned)kt * (64u * 2048u) + wcol, 2048u);
  }
  DI void tail(lds_t st) {
    glds16(rS, st + lw * 1024, (unsigned)((lw * 4 + (lane >> 4)) * 4096 + (lane & 15) * 16), (unsigned)WS_CS1 + pn * 256u);
    glds16(rS, xtra, (unsigned)lane * 16u, (unsigned)WS_RS1 + pm * 1024u);
    const unsigned line = (unsigned)(blockIdx.x * 64 + (lw & 1) * 32 + (lane & 31));
    gtouch((lw & 2) ? rN : rQ, xtra + 4096, (line >> 4) * ((lw & 2) ? 2048u : 6144u) + (line & 15) * 128u, 0u);
  }
};
__global__ __launch_bounds__(512, 2) void k1(const u16* __restrict__ x, const u16* __restrict__ wqkv, const u16* __restrict__ w1,
                                             const u16* __restrict__ w2, char* __restrict__ ws) {
  extern __shared__ __attribute__((aligned(16))) char smem_[];
  lds_t smem = (lds_t)smem_;
  typedef C1 C;
  STAMP0();
  const int w = __builtin_amdgcn_readfirstlane(threadIdx.x >> 6), lane = threadIdx.x & 63;
  const int xcd = blockIdx.x & 7, j = blockIdx.x >> 3;
  const int pm = (xcd & 1) * 8 + (j & 7), pn = (xcd >> 1) * 4 + (j >> 3);
  const int fi = lane & 15, g = lane >> 4;
  const brs_t rW = mk_brs(ws, WS_BYTES);
  LAS f32x2* xst = (LAS f32x2*)(smem + C::XTRA);
  LAS float* xcs = (LAS float*)(smem + C::XTRA + 1024);
  lds_t dump = smem + 2 * C::STAGE;
  if (w >= 4) {
    const int lw = w - 4;
    L1 L;
    L.rA = mk_rsrc(x, S_ * D_ * 2); L.rQ = mk_rsrc(wqkv, D_ * 3 * D_ * 2); L.rW = mk_rsrc(w1, D_ * D_ * 2); L.rS = mk_rsrc(ws, WS_BYTES); L.rN = mk_rsrc(w2, D_ * D_ * 2);
    L.lw = lw; L.lane = lane; L.pm = pm; L.pn = pn; L.xtra = smem + C::XTRA;
    L.va = voff_a(lane, lw, 2048u); L.vq = voff_w(lane, lw, 6144u); L.vw = voff_w(lane, lw, 2048u);
    L.aoff = (unsigned)pm * 128u * 2048u; L.qcol = (unsigned)pn * 128u; L.wcol = (unsigned)pn * 128u;
    produce<C>(smem, 16, L);
    wait_vm<1>();
    {
      f32x4 c = *(const LAS f32x4*)(smem + C::STAGE + lw * 1024 + lane * 16);
#pragma unroll
      for (int e = 0; e < 4; ++e) c[e] = sum_g(c[e]);
      if (g == 0) *(LAS f32x4*)(xcs + lw * 64 + fi * 4) = c;
    }
    wait_lgkm0();
    bar();
    const int wm = lw >> 1, mh = lw & 1;
    f32x4 cs[4];
#pragma unroll
    for (int n = 0; n < 4; ++n) {
      cs[n] = *(const LAS f32x4*)(xcs + n * 16 + g * 4);
#pragma unroll
      for (int i = 1; i < 4; ++i) cs[n] += *(const LAS f32x4*)(xcs + i * 64 + n * 16 + g * 4);
    }
    const int cg = pn >> 2, sh = (cg & 2) ? 2 : 1, dl = (cg & 1) ? -sh : sh;
    const unsigned hs = (unsigned)WS_HS + (pn * 64 + quad_col(lane)) * 2;
#pragma unroll
    for (int mm = 0; mm < 2; ++mm) {
      const int m = mh * 2 + mm, lr0 = wm * 64 + m * 16;
      const f32x2 rs = xst[lr0 + fi];
      unsigned p[4][2];
#pragma unroll
      for (int n = 0; n < 4; ++n) {
        const f32x4 a = *(const LAS f32x4*)(dump + (((wm * 2 + (n >> 1)) * 4 + m) * 2 + (n & 1)) * 1024 + lane * 16);
        float h[4];
#pragma unroll
        for (int r = 0; r < 4; ++r) h[r] = gelu_t(__builtin_fmaf(a[r], rs.x, rs.y * cs[n][r]));
        p[n][0] = cvt_pk(h[0], h[1]); p[n][1] = cvt_pk(h[2], h[3]);
      }
      u32x4 a = pair_swap(p[0][0], p[0][1], p[1][0], p[1][1]), b = pair_swap(p[2][0], p[2][1], p[3][0], p[3][1]);
      row_xchg(a, b);
      const int d0 = pm * 128 + lr0 + (fi & 7) + dl, d1 = d0 + 8;
      if (d0 >= 0) st16(rW, hs + d0 * 2048, a);
      if (d1 < S_) st16(rW, hs + d1 * 2048, b);
    }
    if (dl > 0 ? (pm == 0 && lw == 0) : (pm == 15 && lw == 3)) {
      if ((fi & 7) < sh) { const int zr = dl > 0 ? (fi & 7) : S_ - 1 - (fi & 7); const u32x4 zz = {0u, 0u, 0u, 0u}; st16(rW, hs + zr * 2048, zz); }
    }
    STAMP_END(0, 2);
    return;
  }
  const int wm = w >> 1, wn = w & 1;
  unsigned boff[2];
  boff[0] = 16384 + roff_w(lane, 2 * wn); boff[1] = 16384 + roff_w(lane, 2 * wn + 1);
  f32x4 acc[4][6];
  consume<C, P1>(smem, 16, acc, wm, lane, boff);
  STAMP(0, 1);
#pragma unroll
  for (int m = 0; m < 4; ++m)
#pragma unroll
    for (int n = 0; n < 2; ++n) *(LAS f32x4*)(dump + ((w * 4 + m) * 2 + n) * 1024 + lane * 16) = acc[m][4 + n];
  wait_lgkm0();
  bar();
  {
    const int head = pn * 2 + wn, part = pm * 2 + wm;
    f32x4 kv[2][2], z[2];
#pragma unroll
    for (int T = 0; T < 2; ++T) { z[T] = (f32x4){0.f, 0.f, 0.f, 0.f}; kv[T][0] = z[T]; kv[T][1] = z[T]; }
    const u32x4 onesv = {ONES, ONES, ONES, ONES};
#pragma unroll
    for (int mp = 0; mp < 2; ++mp) {
      u32x4 pk[2], vv[2];
#pragma unroll
      for (int T = 0; T < 2; ++T) {
        const f32x4 a = acc[2 * mp][T], b = acc[2 * mp + 1][T];
        pk[T].x = cvt_pk(phi_f(a[0]), phi_f(a[1])); pk[T].y = cvt_pk(phi_f(a[2]), phi_f(a[3]));
        pk[T].z = cvt_pk(phi_f(b[0]), phi_f(b[1])); pk[T].w = cvt_pk(phi_f(b[2]), phi_f(b[3]));
        const f32x4 c = acc[2 * mp][2 + T], d = acc[2 * mp + 1][2 + T];
        vv[T].x = cvt_pk(c[0], c[1]); vv[T].y = cvt_pk(c[2], c[3]); vv[T].z = cvt_pk(d[0], d[1]); vv[T].w = cvt_pk(d[2], d[3]);
      }
#pragma unroll
      for (int T = 0; T < 2; ++T) {
#pragma unroll
        for (int U = 0; U < 2; ++U)
          kv[T][U] = __builtin_amdgcn_mfma_f32_16x16x32_bf16(__builtin_bit_cast(bf16x8, pk[T]), __builtin_bit_cast(bf16x8, vv[U]), kv[T][U], 0, 0, 0);
        z[T] = __builtin_amdgcn_mfma_f32_16x16x32_bf16(__builtin_bit_cast(bf16x8, pk[T]), __builtin_bit_cast(bf16x8, onesv), z[T], 0, 0, 0);
      }
    }
    const unsigned kp = (unsigned)WS_KVP + (head * 32 + part) * 4096 + lane * 16;
#pragma unroll
    for (int T = 0; T < 2; ++T)
#pragma unroll
      for (int U = 0; U < 2; ++U) st16f(rW, kp + (T * 2 + U) * 1024, kv[T][U]);
    if (fi == 0) {
#pragma unroll
      for (int T = 0; T < 2; ++T) st16f(rW, (unsigned)WS_ZP + (head * 32 + part) * 128 + T * 64 + g * 16, z[T]);
    }
  }
  STAMP_END(0, 2);
}

typedef Cfg<2, 0, 4, 4> C2;
struct P2 {
  static constexpr int NBASE = 4;
  static constexpr bool trans(int) { return true; }
  static constexpr bool narrow(int) { return false; }
  static constexpr int base(int i) { return i; }
  static constexpr int imm(int) { return 0; }
};
struct L2 {
  static constexpr int NTAIL = 8;
  rsrc_t rA, rB, rN; unsigned va, vb, aoff, lda2, ldb2, bcol, noff; int lw, lane;
  DI void issue(lds_t st, int kt) {
    issue_a(rA, st, lw, va, aoff + (unsigned)kt * 128u, lda2);
    issue_w(rB, st + 16384, lw, vb, (unsigned)kt * 64u * ldb2 + bcol, ldb2);
    issue_w(rB, st + 16384 + 8192, lw, vb, (unsigned)kt * 64u * ldb2 + bcol + 128u, ldb2);
  }
  DI void tail(lds_t st) {
#pragma unroll
    for (int i = 0; i < 8; ++i) glds16(rN, st + (lw * 8 + i) * 1024, (unsigned)(lane >> 3) * 7168u + (lane & 7) * 16u, noff + i * (8u * 7168u));
  }
};
__global__ __launch_bounds__(512, 2) void k2(const u16* __restrict__ x, const u16* __restrict__ wqkv, const u16* __restrict__ w2,
                                             const u16* __restrict__ w3, char* __restrict__ ws) {
  extern __shared__ __attribute__((aligned(16))) char smem_[];
  lds_t smem = (lds_t)smem_;
  typedef C2 C;
  STAMP0();
  const int w = __builtin_amdgcn_readfirstlane(threadIdx.x >> 6), lane = threadIdx.x & 63;
  const int xcd = blockIdx.x & 7, j = blockIdx.x >> 3;
  const bool isq = xcd < 4;
  const int pm = (xcd & 1) * 8 + (j & 7), pn = ((xcd >> 1) & 1) * 4 + (j >> 3);
  const int fi = lane & 15, g = lane >> 4;
  const brs_t rW = mk_brs(ws, WS_BYTES);
  if (w >= 4) {
    const int lw = w - 4;
    L2 L;
    L.rA = mk_rsrc(isq ? (const void*)x : (const void*)(ws + WS_HS), S_ * D_ * 2);
    L.rB = isq ? mk_rsrc(wqkv, D_ * 3 * D_ * 2) : mk_rsrc(w2, D_ * D_ * 2);
    L.rN = mk_rsrc(w3, D_ * F_ * 2);
    L.lw = lw; L.lane = lane; L.lda2 = 2048u; L.ldb2 = isq ? 6144u : 2048u;
    L.va = voff_a(lane, lw, 2048u); L.vb = voff_w(lane, lw, L.ldb2);
    L.aoff = (unsigned)pm * 128u * 2048u; L.bcol = (unsigned)pn * 256u;
    const int u = blockIdx.x < 224 ? blockIdx.x : blockIdx.x - 224, slab = u % 56, rg = (u / 56) * 4 + lw;
    L.noff = (unsigned)rg * (64u * 7168u) + slab * 128u;
    produce<C>(smem, 16, L);
    wait_vm<0>();
    float c[8];
#pragma unroll
    for (int e = 0; e < 8; ++e) c[e] = 0.f;
#pragma unroll
    for (int i = 0; i < 8; ++i) {
      const u32x4 v = *(const LAS u32x4*)(smem + (lw * 8 + i) * 1024 + lane * 16);
#pragma unroll
      for (int e = 0; e < 4; ++e) { c[2 * e] += bf_lo(v[e]); c[2 * e + 1] += bf_hi(v[e]); }
    }
#pragma unroll
    for (int e = 0; e < 8; ++e) c[e] = sum_r8(c[e]);
    if (lane < 8 && blockIdx.x < 224) {
      const unsigned o = (unsigned)WS_CS3 + (rg * F_ + slab * 64 + lane * 8) * 4;
      st16f(rW, o, (f32x4){c[0], c[1], c[2], c[3]}); st16f(rW, o + 16, (f32x4){c[4], c[5], c[6], c[7]});
    }
    STAMP_END(1, 2);
    return;
  }
  const int wm = w >> 1, wn = w & 1;
  const unsigned eoff = (unsigned)((pm * 128 + wm * 64 + (fi & 7)) * D_ + pn * 128 + wn * 64 + quad_col(lane)) * 2;
  u32x4 xr[4][2];
  if (!isq) {
#pragma unroll
    for (int m = 0; m < 4; ++m)
#pragma unroll
      for (int p = 0; p < 2; ++p) xr[m][p] = *(const u32x4*)((const char*)x + eoff + (m * 16 + p * 8) * 2048);
  }
  const int kh = blockIdx.x >> 3, kTU = (blockIdx.x >> 1) & 3, khalf = blockIdx.x & 1;
  LAS f32x4* red = (LAS f32x4*)(smem + C::XTRA);
  {
    const char* p = ws + WS_KVP + (size_t)(((kh * 32 + w * 8 + (lane >> 5)) * 4 + kTU) * 1024 + (khalf * 32 + (lane & 31)) * 16);
    f32x4 s = *(const f32x4*)p;
#pragma unroll
    for (int i = 1; i < 4; ++i) s += *(const f32x4*)(p + i * 8192);
    red[w * 64 + lane] = s;
    if (w == 3 && (blockIdx.x & 7) == 0) {
      const char* zp = ws + WS_ZP + (size_t)kh * 4096 + lane * 16;
      f32x4 z = *(const f32x4*)zp;
#pragma unroll
      for (int i = 1; i < 4; ++i) z += *(const f32x4*)(zp + i * 1024);
#pragma unroll
      for (int e = 0; e < 4; ++e) z[e] = sum_r8(z[e]);
      u32x2 v; v.x = cvt_pk(z[0], z[1]); v.y = cvt_pk(z[2], z[3]);
      if (lane < 8) st8(rW, (unsigned)WS_ZF + kh * 64 + lane * 8, v);
    }
  }
  unsigned boff[4];
#pragma unroll
  for (int i = 0; i < 4; ++i) boff[i] = 16384 + wn * 8192 + roff_w(lane, i);
  f32x4 acc[4][4];
  consume<C, P2>(smem, 16, acc, wm, lane, boff);
  STAMP(1, 1);
  if (isq) {
#pragma unroll
    for (int m = 0; m < 4; ++m) {
      unsigned p[4][2];
#pragma unroll
      for (int n = 0; n < 4; ++n) { p[n][0] = cvt_pk(phi_f(acc[m][n][0]), phi_f(acc[m][n][1])); p[n][1] = cvt_pk(phi_f(acc[m][n][2]), phi_f(acc[m][n][3])); }
      store_quad(rW, (unsigned)WS_PQ + eoff + m * 32768, 16384, p);
    }
  } else {
#pragma unroll
    for (int m = 0; m < 4; ++m) {
      float s1 = 0.f, s2 = 0.f;
      unsigned p[4][2];
      unpack_quad(xr[m][0], xr[m][1], p);
#pragma unroll
      for (int n = 0; n < 4; ++n) {
        p[n][0] = cvt_pk(acc[m][n][0] + bf_lo(p[n][0]), acc[m][n][1] + bf_hi(p[n][0]));
        p[n][1] = cvt_pk(acc[m][n][2] + bf_lo(p[n][1]), acc[m][n][3] + bf_hi(p[n][1]));
        s1 = dot2(p[n][0], ONES, s1); s1 = dot2(p[n][1], ONES, s1);
        s2 = dot2(p[n][0], p[n][0], s2); s2 = dot2(p[n][1], p[n][1], s2);
      }
      store_quad(rW, (unsigned)WS_X1 + eoff + m * 32768, 16384, p);
      s1 = sum_g(s1); s2 = sum_g(s2);
      if (g == 0) { u32x2 v; v.x = __float_as_uint(s1); v.y = __float_as_uint(s2); st8(rW, (unsigned)WS_ST + (pm * 128 + wm * 64 + m * 16 + fi) * 128 + (pn * 2 + wn) * 8, v); }
    }
  }
  if (w == 0 && lane < 32) {
    f32x4 s = red[lane];
#pragma unroll
    for (int i = 1; i < 8; ++i) s += red[i * 32 + lane];
    const int l = khalf * 32 + lane, gg = l >> 4, T = kTU >> 1, U = kTU & 1;
    u32x2 v; v.x = cvt_pk(s[0], s[1]); v.y = cvt_pk(s[2], s[3]);
    st8(rW, (unsigned)WS_KVF + (kh * 2 + U) * 1024 + ((2 * T + (gg >> 1)) * 16 + (l & 15)) * 16 + (gg & 1) * 8, v);
  }
  STAMP_END(1, 2);
}

typedef Cfg<3, 1, 7, 3> C3;
struct P3 {
  static constexpr int NBASE = 7;
  static constexpr bool trans(int) { return true; }
  static constexpr bool narrow(int i) { return i == 6; }
  static constexpr int base(int i) { return i; }
  static constexpr int imm(int) { return 0; }
};
struct L3 {
  static constexpr int NTAIL = 11;
  rsrc_t rA, rB, rS, rO, rF; unsigned va, vb, vn, aoff, wcol, ncol; int lw, lane, pm, pn; lds_t xtra;
  DI void issue(lds_t st, int kt) {
    issue_a(rA, st, lw, va, aoff + (unsigned)kt * 128u, 2048u);
    const unsigned kb = (unsigned)kt * (64u * 7168u);
#pragma unroll
    for (int b = 0; b < 3; ++b) issue_w(rB, st + 16384 + b * 8192, lw, vb, kb + wcol + b * 128u, 7168u);
    glds16(rB, st + 16384 + 24576 + lw * 1024, vn, kb + ncol + (unsigned)lw * (16u * 7168u));
  }
  DI void tail(lds_t st) {
    st += lw * 9216;
    const unsigned l16 = (unsigned)lane * 16u;
#pragma unroll
    for (int i = 0; i < 4; ++i) glds16(rS, st + i * 1024, l16, (unsigned)WS_ST + (pm * 128 + lw * 32) * 128u + i * 1024u);
#pragma unroll
    for (int i = 0; i < 4; ++i) glds16(rS, st + 4096 + i * 1024, l16, (unsigned)WS_CS3 + ((lw * 4 + i) * F_ + pn * 224) * 4u);
    glds16(rS, st + 8192, l16, (unsigned)WS_G + pm * 1024u);
    const unsigned lo = (unsigned)((blockIdx.x * 4 + lw) * 16 + (lane & 15)), lf = (unsigned)((blockIdx.x * 4 + lw) * 56 + (lane < 56 ? lane : 55));
    gtouch(rF, xtra + 6144, lf * 128u, 0u);
    gtouch(rO, xtra + 6144, lo * 128u, 0u);
  }
};
__global__ __launch_bounds__(512, 2) void k3(const u16* __restrict__ w3, const u16* __restrict__ wo, const u16* __restrict__ w4, char* __restrict__ ws) {
  extern __shared__ __attribute__((aligned(16))) char smem_[];
  lds_t smem = (lds_t)smem_;
  typedef C3 C;
  STAMP0();
  const int w = __builtin_amdgcn_readfirstlane(threadIdx.x >> 6), lane = threadIdx.x & 63;
  const int xcd = blockIdx.x & 7, j = blockIdx.x >> 3;
  const int pm = (xcd & 1) * 8 + (j & 7), pn = (xcd >> 1) * 4 + (j >> 3);
  const int fi = lane & 15, g = lane >> 4;
  const brs_t rW = mk_brs(ws, WS_BYTES);
  const int wc0 = (pn & 1) ? 32 : 0, nc0 = (pn & 1) ? 0 : 192;
  LAS f32x4* rst = (LAS f32x4*)(smem + C::XTRA);
  LAS float* xcs = (LAS float*)(smem + C::XTRA + 2048);
  lds_t dump = smem + C::STAGE + 36864;
  const unsigned urow = (unsigned)WS_AU + ((pm * 128) * LDAU + 1024 + pn * 224) * 2;
  if (w >= 4) {
    const int lw = w - 4;
    L3 L;
    L.rA = mk_rsrc(ws + WS_X1, S_ * D_ * 2); L.rB = mk_rsrc(w3, D_ * F_ * 2); L.rS = mk_rsrc(ws, WS_BYTES);
    L.rO = mk_rsrc(wo, D_ * D_ * 2); L.rF = mk_rsrc(w4, F_ * D_ * 2);
    L.lw = lw; L.lane = lane; L.pm = pm; L.pn = pn; L.xtra = smem + C::XTRA;
    L.va = voff_a(lane, lw, 2048u); L.vb = voff_w(lane, lw, 7168u); L.vn = voff_n(lane, 7168u);
    L.aoff = (unsigned)pm * 128u * 2048u; L.wcol = (unsigned)(pn * 224 + wc0) * 2u; L.ncol = (unsigned)(pn * 224 + nc0) * 2u;
    produce<C>(smem, 16, L);
    wait_vm<2>();
    {
      lds_t st = smem + C::STAGE + lw * 9216;
      f32x4 s = *(const LAS f32x4*)(st + lane * 64), c = *(const LAS f32x4*)(st + 4096 + lane * 16);
#pragma unroll
      for (int i = 1; i < 4; ++i) { s += *(const LAS f32x4*)(st + lane * 64 + i * 16); c += *(const LAS f32x4*)(st + 4096 + i * 1024 + lane * 16); }
      const int lr = lw * 32 + (lane >> 1);
      const float omg = *(const LAS float*)(st + 8192 + lr * 8 + 4);
      float s1 = s[0] + s[2], s2 = s[1] + s[3];
      s1 += __shfl_xor(s1, 1); s2 += __shfl_xor(s2, 1);
      const float mean = s1 * (1.f / 1024.f);
      const float var = fmaxf(s2 * (1.f / 1024.f) - mean * mean, 0.f);
      const float rstd = 1.f / sqrtf(var + 1e-5f);
      if (!(lane & 1)) rst[lr] = (f32x4){rstd, -mean * rstd, omg, 0.f};
      *(LAS f32x4*)(xcs + lw * 256 + lane * 4) = c;
    }
    wait_lgkm0();
    bar();
    const int wm = lw >> 1, mh = lw & 1;
    f32x4 cs[6];
#pragma unroll
    for (int n = 0; n < 6; ++n) {
      const int c0 = (n < 4 ? wc0 + 128 + n * 16 : nc0 + (n - 4) * 16) + g * 4;
      cs[n] = *(const LAS f32x4*)(xcs + c0);
#pragma unroll
      for (int i = 1; i < 4; ++i) cs[n] += *(const LAS f32x4*)(xcs + i * 256 + c0);
    }
#pragma unroll
    for (int mm = 0; mm < 2; ++mm) {
      const int m = mh * 2 + mm, lr0 = wm * 64 + m * 16;
      const f32x4 rs = rst[lr0 + fi];
      unsigned p[4][2], q[2][2];
#pragma unroll
      for (int n = 0; n < 6; ++n) {
        const int sw = n < 4 ? (n >> 1) : (n - 4), si = n < 4 ? (n & 1) : 2;
        const f32x4 a = *(const LAS f32x4*)(dump + (((wm * 2 + sw) * 4 + m) * 3 + si) * 1024 + lane * 16);
        float h[4];
#pragma unroll
        for (int r = 0; r < 4; ++r) h[r] = rs[2] * gelu_t(__builtin_fmaf(a[r], rs[0], rs[1] * cs[n][r]));
        if (n < 4) { p[n][0] = cvt_pk(h[0], h[1]); p[n][1] = cvt_pk(h[2], h[3]); }
        else { q[n - 4][0] = cvt_pk(h[0], h[1]); q[n - 4][1] = cvt_pk(h[2], h[3]); }
      }
      store_quad(rW, urow + ((lr0 + (fi & 7)) * LDAU + wc0 + 128 + quad_col(lane)) * 2, 8 * LDAU * 2, p);
      st16(rW, urow + ((lr0 + fi) * LDAU + nc0 + (g & 1) * 16 + (g & 2) * 4) * 2, pair_swap(q[0][0], q[0][1], q[1][0], q[1][1]));
    }
    STAMP_END(2, 2);
    return;
  }
  const int wm = w >> 1, wn = w & 1;
  {
    const int head = pn * 2 + wn, r0 = pm * 128 + wm * 64 + fi;
    const bf16x8 kf0 = *(const bf16x8*)(ws + WS_KVF + (size_t)(head * 2) * 1024 + lane * 16);
    const bf16x8 kf1 = *(const bf16x8*)(ws + WS_KVF + (size_t)(head * 2 + 1) * 1024 + lane * 16);
    const bf16x8 zf = *(const bf16x8*)(ws + WS_ZF + (size_t)head * 64 + g * 16);
    bf16x8 pq[4]; float gg[4];
#pragma unroll
    for (int m = 0; m < 4; ++m) {
      pq[m] = *(const bf16x8*)((const u16*)(ws + WS_PQ) + (size_t)(r0 + m * 16) * D_ + head * 32 + g * 8);
      gg[m] = *(const float*)(ws + WS_G + (size_t)(r0 + m * 16) * 8);
    }
#pragma unroll
    for (int m = 0; m < 4; ++m) {
      const f32x4 zero = {0.f, 0.f, 0.f, 0.f};
      const f32x4 n0 = __builtin_amdgcn_mfma_f32_16x16x32_bf16(kf0, pq[m], zero, 0, 0, 0);
      const f32x4 n1 = __builtin_amdgcn_mfma_f32_16x16x32_bf16(kf1, pq[m], zero, 0, 0, 0);
      const f32x4 dn = __builtin_amdgcn_mfma_f32_16x16x32_bf16(zf, pq[m], zero, 0, 0, 0);
      const float sc = gg[m] / (dn[0] + 1e-6f);
      const u32x4 v = pair_swap(cvt_pk(n0[0] * sc, n0[1] * sc), cvt_pk(n0[2] * sc, n0[3] * sc), cvt_pk(n1[0] * sc, n1[1] * sc), cvt_pk(n1[2] * sc, n1[3] * sc));
      st16(rW, (unsigned)WS_AU + ((r0 + m * 16) * LDAU + head * 32 + (g & 1) * 16 + (g & 2) * 4) * 2, v);
    }
  }
  unsigned boff[7];
#pragma unroll
  for (int i = 0; i < 4; ++i) boff[i] = 16384 + wn * 8192 + roff_w(lane, i);
  boff[4] = 16384 + 16384 + roff_w(lane, 2 * wn); boff[5] = 16384 + 16384 + roff_w(lane, 2 * wn + 1);
  boff[6] = 16384 + 24576 + roff_n(lane, wn);
  f32x4 acc[4][7];
  consume<C, P3>(smem, 16, acc, wm, lane, boff);
  STAMP(2, 1);
#pragma unroll
  for (int m = 0; m < 4; ++m)
#pragma unroll
    for (int n = 0; n < 3; ++n) *(LAS f32x4*)(dump + ((w * 4 + m) * 3 + n) * 1024 + lane * 16) = acc[m][4 + n];
  wait_lgkm0();
  bar();
  f32x4 cs[4];
#pragma unroll
  for (int n = 0; n < 4; ++n) {
    const int c0 = wc0 + wn * 64 + n * 16 + g * 4;
    cs[n] = *(const LAS f32x4*)(xcs + c0);
#pragma unroll
    for (int i = 1; i < 4; ++i) cs[n] += *(const LAS f32x4*)(xcs + i * 256 + c0);
  }
#pragma unroll
  for (int m = 0; m < 4; ++m) {
    const int lr0 = wm * 64 + m * 16;
    const f32x4 rs = rst[lr0 + fi];
    unsigned p[4][2];
#pragma unroll
    for (int n = 0; n < 4; ++n) {
      float h[4];
#pragma unroll
      for (int r = 0; r < 4; ++r) h[r] = rs[2] * gelu_t(__builtin_fmaf(acc[m][n][r], rs[0], rs[1] * cs[n][r]));
      p[n][0] = cvt_pk(h[0], h[1]); p[n][1] = cvt_pk(h[2], h[3]);
    }
    store_quad(rW, urow + ((lr0 + (fi & 7)) * LDAU + wc0 + wn * 64 + quad_col(lane)) * 2, 8 * LDAU * 2, p);
  }
  STAMP_END(2, 2);
}

typedef Cfg<2, 0, 4, 4> C4;
struct L4 {
  static constexpr int NTAIL = 0;
  rsrc_t rA, rO, rW; unsigned va, vb, aoff, bcol; int lw, kt0;
  DI void issue(lds_t st, int t) {
    const int kt = kt0 + t;
    issue_a(rA, st, lw, va, aoff + (unsigned)kt * 128u, LDAU * 2u);
    const bool o = kt < 16;
    rsrc_t rB; rB.x = o ? rO.x : rW.x; rB.y = o ? rO.y : rW.y; rB.z = o ? rO.z : rW.z; rB.w = rO.w;
    const unsigned kb = (unsigned)(o ? kt : kt - 16) * (64u * 2048u) + bcol;
    issue_w(rB, st + 16384, lw, vb, kb, 2048u);
    issue_w(rB, st + 16384 + 8192, lw, vb, kb + 128u, 2048u);
  }
  DI void tail(lds_t) {}
};
__global__ __launch_bounds__(512, 2) void k4(const u16* __restrict__ wo, const u16* __restrict__ w4, char* __restrict__ ws, u16* __restrict__ out) {
  extern __shared__ __attribute__((aligned(16))) char smem_[];
  lds_t smem = (lds_t)smem_;
  typedef C4 C;
  STAMP0();
  const int w = __builtin_amdgcn_readfirstlane(threadIdx.x >> 6), lane = threadIdx.x & 63;
  const int xcd = blockIdx.x & 7, j = blockIdx.x >> 3;
  const int hf = xcd >> 2;
  const int pm = (xcd & 1) * 8 + (j & 7), pn = ((xcd >> 1) & 1) * 4 + (j >> 3);
  const int fi = lane & 15;
  const brs_t rW = mk_brs(ws, WS_BYTES);
  if (w >= 4) {
    const int lw = w - 4;
    L4 L;
    L.rA = mk_rsrc(ws + WS_AU, S_ * LDAU * 2); L.rO = mk_rsrc(wo, D_ * D_ * 2); L.rW = mk_rsrc(w4, F_ * D_ * 2);
    L.lw = lw; L.kt0 = hf * 36; L.va = voff_a(lane, lw, LDAU * 2u); L.vb = voff_w(lane, lw, 2048u);
    L.aoff = (unsigned)pm * 128u * (LDAU * 2u); L.bcol = (unsigned)pn * 256u;
    produce<C>(smem, 36, L);
    STAMP_END(3, 2);
    return;
  }
  const int wm = w >> 1, wn = w & 1;
  const int row0 = pm * 128 + wm * 64 + hf * 32;
  const unsigned eoff = (unsigned)((row0 + (fi & 7)) * D_ + pn * 128 + wn * 64 + quad_col(lane)) * 2;
  u32x4 xr[2][2]; float omg[2];
#pragma unroll
  for (int m = 0; m < 2; ++m) {
#pragma unroll
    for (int p = 0; p < 2; ++p) xr[m][p] = *(const u32x4*)(ws + WS_X1 + eoff + (m * 16 + p * 8) * 2048);
    omg[m] = *(const float*)(ws + WS_G + (size_t)(row0 + m * 16 + fi) * 8 + 4);
  }
  unsigned boff[4];
#pragma unroll
  for (int i = 0; i < 4; ++i) boff[i] = 16384 + wn * 8192 + roff_w(lane, i);
  f32x4 acc[4][4];
  consume<C, P2>(smem, 36, acc, wm, lane, boff);
  STAMP(3, 1);
  f32x4 own[2][4];
  {
    const unsigned ob = (unsigned)WS_XCH + (blockIdx.x * 4 + w) * 8192 + lane * 16;
#pragma unroll
    for (int m = 0; m < 2; ++m)
#pragma unroll
      for (int n = 0; n < 4; ++n) {
        f32x4 snd;
#pragma unroll
        for (int r = 0; r < 4; ++r) { snd[r] = hf ? acc[m][n][r] : acc[2 + m][n][r]; own[m][n][r] = hf ? acc[2 + m][n][r] : acc[m][n][r]; }
        st16f(rW, ob + (m * 4 + n) * 1024, snd);
      }
    wait_vm<0>();
    STAMP(3, 3);
    bar();
    if (threadIdx.x == 0) __builtin_amdgcn_raw_buffer_store_b32(1u, rW, (int)((unsigned)WS_FLAG + blockIdx.x * 128), 0, 16);
  }
  {
    const char* fp = ws + WS_FLAG + (size_t)(blockIdx.x ^ 4) * 128;
    unsigned v = 0;
    for (int it = 0; it < (1 << 18); ++it) {
      asm volatile("global_load_dword %0, %1, off sc1\n\ts_waitcnt vmcnt(0)" : "=v"(v) : "v"(fp) : "memory");
      if (__builtin_amdgcn_readfirstlane(v) != 0u) break;
      __builtin_amdgcn_s_sleep(1);
    }
    STAMP(3, 4);
    const unsigned ib = (unsigned)WS_XCH + ((blockIdx.x ^ 4) * 4 + w) * 8192 + lane * 16;
    f32x4 o[2][4];
#pragma unroll
    for (int m = 0; m < 2; ++m)
#pragma unroll
      for (int n = 0; n < 4; ++n) o[m][n] = __builtin_bit_cast(f32x4, __builtin_amdgcn_raw_buffer_load_b128(rW, (int)(ib + (m * 4 + n) * 1024), 0, 16));
    const brs_t rO = mk_brs(out, S_ * D_ * 2);
#pragma unroll
    for (int m = 0; m < 2; ++m) {
      unsigned p[4][2];
      unpack_quad(xr[m][0], xr[m][1], p);
      const float q = omg[m];
#pragma unroll
      for (int n = 0; n < 4; ++n) {
        const f32x4 a = own[m][n] + o[m][n];
        p[n][0] = cvt_pk(__builtin_fmaf(q, bf_lo(p[n][0]), a[0]), __builtin_fmaf(q, bf_hi(p[n][0]), a[1]));
        p[n][1] = cvt_pk(__builtin_fmaf(q, bf_lo(p[n][1]), a[2]), __builtin_fmaf(q, bf_hi(p[n][1]), a[3]));
      }
      store_quad(rO, eoff + m * 32768, 16384, p);
    }
  }
  STAMP_END(3, 2);
}

extern "C" void kernel_launch(void* const* d_in, const int* in_sizes, int n_in, void* d_out, int out_size, void* d_ws, size_t ws_size,
                              hipStream_t stream) {
  const u16* x = (const u16*)d_in[0];
  const u16* wqkv = (const u16*)d_in[1];
  const u16* wo = (const u16*)d_in[2];
  const u16* w1 = (const u16*)d_in[4];
  const u16* w2 = (const u16*)d_in[5];
  const u16* w3 = (const u16*)d_in[6];
  const u16* w4 = (const u16*)d_in[7];
  const u16* gw = (const u16*)d_in[8];
  char* ws = (char*)d_ws;
  (void)hipFuncSetAttribute((const void*)k1, hipFuncAttributeMaxDynamicSharedMemorySize, C1::LDS);
  (void)hipFuncSetAttribute((const void*)k2, hipFuncAttributeMaxDynamicSharedMemorySize, C2::LDS);
  (void)hipFuncSetAttribute((const void*)k3, hipFuncAttributeMaxDynamicSharedMemorySize, C3::LDS);
  (void)hipFuncSetAttribute((const void*)k4, hipFuncAttributeMaxDynamicSharedMemorySize, C4::LDS);
  k0<<<256, 512, 0, stream>>>(x, wqkv, w1, gw, ws);
  k1<<<256, 512, C1::LDS, stream>>>(x, wqkv, w1, w2, ws);
  k2<<<256, 512, C2::LDS, stream>>>(x, wqkv, w2, w3, ws);
  k3<<<256, 512, C3::LDS, stream>>>(w3, wo, w4, ws);
  k4<<<256, 512, C4::LDS, stream>>>(wo, w4, ws, (u16*)d_out);
}
```

```cpp
#include <hip/hip_runtime.h>

typedef unsigned short u16;
typedef unsigned long long u64;
typedef short bf16x8 __attribute__((ext_vector_type(8)));
typedef short s16x4 __attribute__((ext_vector_type(4)));
typedef float f32x4 __attribute__((ext_vector_type(4)));
typedef float f32x2 __attribute__((ext_vector_type(2)));
typedef unsigned u32x4 __attribute__((ext_vector_type(4)));
typedef unsigned u32x2 __attribute__((ext_vector_type(2)));
typedef __bf16 bf16x2_t __attribute__((ext_vector_type(2)));
#define LAS __attribute__((address_space(3)))
typedef LAS char* lds_t;
typedef u32x4 rsrc_t;
#define DI __device__ __forceinline__

constexpr int S_ = 2048, D_ = 1024, F_ = 3584;
constexpr int LDAU = 4608 + 64;
constexpr size_t MB = 1u << 20;
constexpr size_t WS_HS = 0, WS_PQ = 4 * MB, WS_X1 = 8 * MB, WS_AU = 12 * MB, WS_KVP = 32 * MB, WS_ZP = 36 * MB,
                 WS_KVF = 36 * MB + 512 * 1024, WS_ZF = 36 * MB + 768 * 1024, WS_ST = 37 * MB, WS_G = 37 * MB + 512 * 1024,
                 WS_RS1 = 37 * MB + 640 * 1024, WS_CS1 = 37 * MB + 768 * 1024, WS_FLAG = 38 * MB, WS_CS3 = 39 * MB, WS_XCH = 40 * MB;

DI rsrc_t mk_rsrc(const void* p, unsigned bytes) {
  u64 a = (u64)p; rsrc_t r; r.x = (unsigned)a; r.y = (unsigned)(a >> 32) & 0xffffu; r.z = bytes; r.w = 0x00020000u; return r;
}
DI void glds16(rsrc_t r, lds_t dst, unsigned voff, unsigned soff) {
  asm volatile("s_mov_b32 m0, %0\n\ts_nop 0\n\tbuffer_load_dwordx4 %1, %2, %3 offen lds" ::"s"((unsigned)(size_t)dst), "v"(voff), "s"(r), "s"(soff) : "memory");
}
DI void gtouch(rsrc_t r, lds_t dst, unsigned voff, unsigned soff) {
  asm volatile("s_mov_b32 m0, %0\n\ts_nop 0\n\tbuffer_load_dword %1, %2, %3 offen lds" ::"s"((unsigned)(size_t)dst), "v"(voff), "s"(r), "s"(soff) : "memory");
}
template <int N> DI void wait_vm() { asm volatile("s_waitcnt vmcnt(%0)" ::"n"(N) : "memory"); }
DI void wait_lgkm0() { asm volatile("" ::: "memory"); __builtin_amdgcn_s_waitcnt(0xC07F); asm volatile("" ::: "memory"); }
DI void bar() { asm volatile("" ::: "memory"); __builtin_amdgcn_s_barrier(); asm volatile("" ::: "memory"); }
DI unsigned cvt_pk(float lo, float hi) { f32x2 v = {lo, hi}; bf16x2_t r = __builtin_convertvector(v, bf16x2_t); return __builtin_bit_cast(unsigned, r); }
DI float bf_lo(unsigned u) { return __builtin_bit_cast(float, u << 16); }
DI float bf_hi(unsigned u) { return __builtin_bit_cast(float, u & 0xffff0000u); }
DI float rbf(float f) { return bf_lo(cvt_pk(f, 0.f) & 0xffffu); }
DI float dot2(unsigned a, unsigned b, float c) { return __builtin_amdgcn_fdot2_f32_bf16(__builtin_bit_cast(bf16x2_t, a), __builtin_bit_cast(bf16x2_t, b), c, false); }
constexpr unsigned ONES = 0x3f803f80u;
typedef __amdgpu_buffer_rsrc_t brs_t;
constexpr unsigned WS_BYTES = 48u << 20;
DI brs_t mk_brs(void* p, unsigned bytes) { return __builtin_amdgcn_make_buffer_rsrc(p, (short)0, (int)bytes, 0x00020000); }
#define CT 0
#define SAUX 16
DI void st16(brs_t r, unsigned off, u32x4 v) { __builtin_amdgcn_raw_buffer_store_b128(v, r, (int)off, 0, SAUX); }
DI void st8(brs_t r, unsigned off, u32x2 v) { __builtin_amdgcn_raw_buffer_store_b64(v, r, (int)off, 0, SAUX); }
DI void st16f(brs_t r, unsigned off, f32x4 v) { __builtin_amdgcn_raw_buffer_store_b128(__builtin_bit_cast(u32x4, v), r, (int)off, 0, 16); }
#define STAMP(kid, i) do {} while (0)
#define STAMP_END(kid, i) do {} while (0)
#define STAMP0() do {} while (0)
#define STAMPC(kid) do {} while (0)
#define CODE_TOUCH(E, i) do { u64 pc_; unsigned len_; asm volatile("s_getpc_b64 %0\n1:\n\ts_mov_b32 %1, " E "-1b" : "=s"(pc_), "=s"(len_)); \
    const unsigned o_ = (unsigned)(i) * 128u; if (o_ < len_) { unsigned t_; asm volatile("global_load_dword %0, %1, %2" : "=v"(t_) : "v"(o_), "s"(pc_) : "memory"); ct_ = t_; } } while (0)
DI float sum_g(float v) {
  unsigned u = __builtin_bit_cast(unsigned, v);
  auto a = __builtin_amdgcn_permlane16_swap(u, u, false, false);
  const unsigned a0 = a[0], a1 = a[1];
  v = __uint_as_float(a0) + __uint_as_float(a1);
  u = __builtin_bit_cast(unsigned, v);
  auto b = __builtin_amdgcn_permlane32_swap(u, u, false, false);
  const unsigned b0 = b[0], b1 = b[1];
  return __uint_as_float(b0) + __uint_as_float(b1);
}
DI float gelu_t(float v) {
  const float e = __builtin_amdgcn_exp2f(v * __builtin_fmaf(v * v, -0.10294323970f, -2.30220819814f));
  return v * __builtin_amdgcn_rcpf(1.f + e);
}
DI float phi_f(float t) { return (t > 0.f ? t + 1.f : __builtin_amdgcn_exp2f(t * 1.44269504089f)) + 1e-6f; }
DI u32x4 pair_swap(unsigned a0, unsigned a1, unsigned b0, unsigned b1) {
  auto r0 = __builtin_amdgcn_permlane16_swap(a0, b0, false, false);
  auto r1 = __builtin_amdgcn_permlane16_swap(a1, b1, false, false);
  u32x4 v; v.x = r0[0]; v.y = r1[0]; v.z = r0[1]; v.w = r1[1]; return v;
}
DI int quad_col(int lane) { const int g = lane >> 4; return ((lane >> 3) & 1) * 32 + (g & 1) * 16 + (g & 2) * 4; }
DI void row_xchg(u32x4& a, u32x4& b) {
#pragma unroll
  for (int e = 0; e < 4; ++e) {
    const unsigned x = a[e], y = b[e];
    a[e] = (unsigned)__builtin_amdgcn_update_dpp((int)x, (int)y, 0x128, 0xf, 0xc, false);
    b[e] = (unsigned)__builtin_amdgcn_update_dpp((int)y, (int)x, 0x128, 0xf, 0x3, false);
  }
}
DI void store_quad(brs_t r, unsigned off  , unsigned pitch8  , const unsigned (&p)[4][2]) {
  u32x4 a = pair_swap(p[0][0], p[0][1], p[1][0], p[1][1]), b = pair_swap(p[2][0], p[2][1], p[3][0], p[3][1]);
  row_xchg(a, b);
  st16(r, off, a); st16(r, off + pitch8, b);
}
DI void unpack_quad(u32x4 a, u32x4 b, unsigned (&p)[4][2]) {
  row_xchg(a, b);
  const u32x4 x = pair_swap(a.x, a.y, a.z, a.w), y = pair_swap(b.x, b.y, b.z, b.w);
  p[0][0] = x.x; p[0][1] = x.y; p[1][0] = x.z; p[1][1] = x.w; p[2][0] = y.x; p[2][1] = y.y; p[3][0] = y.z; p[3][1] = y.w;
}
DI float sum_r8(float v) {
  v += __int_as_float(__builtin_amdgcn_update_dpp(0, __float_as_int(v), 0x128, 0xf, 0xf, false));
  return sum_g(v);
}

template <int NWB_, int NNB_, int NT_, int NS_>
struct Cfg {
  static constexpr int NWB = NWB_, NNB = NNB_, NT = NT_, NS = NS_, MT = 4;
  static constexpr int A_BYTES = 16384, STAGE = A_BYTES + NWB * 8192 + NNB * 4096, XTRA = NS * STAGE, LDS = XTRA + 8192;
  static constexpr int L = 4 + 2 * NWB + NNB;
};
template <class C> struct Frags { bf16x8 a[C::MT]; bf16x8 b[C::NT]; };

DI bf16x8 rd_bfrag(lds_t p, int hi_off) {
  s16x4 lo = __builtin_amdgcn_ds_read_tr16_b64_v4i16((LAS s16x4*)p);
  s16x4 hi = __builtin_amdgcn_ds_read_tr16_b64_v4i16((LAS s16x4*)(p + hi_off));
  return __builtin_shufflevector(lo, hi, 0, 1, 2, 3, 4, 5, 6, 7);
}
template <class C, class P>
DI void read_frags(Frags<C>& F, int ks, const unsigned (&ad)[P::NBASE + 2]) {
#pragma unroll
  for (int i = 0; i < C::NT; ++i) {
    lds_t p = (lds_t)(size_t)ad[P::base(i)];
    F.b[i] = P::narrow(i) ? rd_bfrag(p + ks * 2048, 256) : rd_bfrag(p + P::imm(i) + ks * 4096, 512);
    if (i == 0) {
#pragma unroll
      for (int m = 0; m < C::MT; ++m) F.a[m] = *(const LAS bf16x8*)((lds_t)(size_t)ad[P::NBASE + ks] + m * 2048);
    }
  }
}
template <class C, class P>
DI void mma(f32x4 (&acc)[C::MT][C::NT], const Frags<C>& F) {
#pragma unroll
  for (int n = 0; n < C::NT; ++n)
#pragma unroll
    for (int m = 0; m < C::MT; ++m)
      acc[m][n] = P::trans(n) ? __builtin_amdgcn_mfma_f32_16x16x32_bf16(F.b[n], F.a[m], acc[m][n], 0, 0, 0)
                              : __builtin_amdgcn_mfma_f32_16x16x32_bf16(F.a[m], F.b[n], acc[m][n], 0, 0, 0);
}
template <class C, int NV>
DI void sched_hint() {
  constexpr int NM = C::MT * C::NT, NR = C::MT + 2 * C::NT, PV = (NV + NM - NR - 1) / (NM - NR);
#pragma unroll
  for (int i = 0; i < NM; ++i) {
    __builtin_amdgcn_sched_group_barrier(0x8, 1, 0);
    if (i < NR) __builtin_amdgcn_sched_group_barrier(0x100, 1, 0);
    else if (NV) __builtin_amdgcn_sched_group_barrier(0x2, PV, 0);
  }
  __builtin_amdgcn_sched_barrier(0);
}
template <class C, class P>
DI void consume(lds_t smem, int nt, f32x4 (&acc)[C::MT][C::NT], int wm, int lane, const unsigned (&boff)[P::NBASE]) {
  const int fi = lane & 15, g = lane >> 4, f = (fi >> 1) & 7;
  unsigned ad[P::NBASE + 2];
#pragma unroll
  for (int i = 0; i < P::NBASE; ++i) ad[i] = (unsigned)(size_t)smem + boff[i];
  ad[P::NBASE] = (unsigned)(size_t)smem + (unsigned)((wm * 64 + fi) * 128 + ((g ^ f) * 16));
  ad[P::NBASE + 1] = (unsigned)(size_t)smem + (unsigned)((wm * 64 + fi) * 128 + (((4 + g) ^ f) * 16));
#pragma unroll
  for (int m = 0; m < C::MT; ++m)
#pragma unroll
    for (int n = 0; n < C::NT; ++n) acc[m][n] = (f32x4){0.f, 0.f, 0.f, 0.f};
  Frags<C> F0, F1;
  wait_lgkm0();
  bar();
  read_frags<C, P>(F0, 0, ad);
  int rs = 0;
  for (int t = 0; t < nt - 1; ++t) {
    rs = (rs + 1 == C::NS) ? 0 : rs + 1;
    const unsigned dl = rs ? (unsigned)C::STAGE : (unsigned)(-(C::NS - 1) * C::STAGE);
    __builtin_amdgcn_sched_barrier(0);
    read_frags<C, P>(F1, 1, ad);
    mma<C, P>(acc, F0);
#pragma unroll
    for (int i = 0; i < P::NBASE + 2; ++i) { ad[i] += dl; asm volatile("" : "+v"(ad[i])); }
    sched_hint<C, P::NBASE + 2>();
    wait_lgkm0();
    bar();
    __builtin_amdgcn_sched_barrier(0);
    read_frags<C, P>(F0, 0, ad);
    mma<C, P>(acc, F1);
    sched_hint<C, 0>();
  }
  read_frags<C, P>(F1, 1, ad);
  mma<C, P>(acc, F0);
  sched_hint<C, 0>();
  mma<C, P>(acc, F1);
}
template <class C, class LD>
DI void produce(lds_t smem, int nt, LD& L) {
#pragma unroll
  for (int s = 0; s < C::NS - 1; ++s) L.issue(smem + s * C::STAGE, s);
  wait_vm<(C::NS - 2) * C::L>();
  bar();
  int ws = C::NS - 1;
  for (int t = 0; t < nt - C::NS + 1; ++t) {
    L.issue(smem + ws * C::STAGE, t + C::NS - 1);
    wait_vm<(C::NS - 2) * C::L>();
    ws = (ws + 1 == C::NS) ? 0 : ws + 1;
    bar();
  }
  L.tail(smem + ws * C::STAGE);
  if (C::NS == 4) { wait_vm<C::L + LD::NTAIL>(); bar(); }
  wait_vm<LD::NTAIL>();
  bar();
}
DI unsigned voff_a(int lane, int lw, unsigned lda2) { return (unsigned)(lane >> 3) * lda2 + (unsigned)(((lane & 7) ^ (((lw & 1) * 4 + (lane >> 4)) & 7)) * 16); }
DI unsigned voff_w(int lane, int lw, unsigned ldb2) { return (unsigned)(lane >> 3) * ldb2 + (unsigned)(((lane & 7) ^ (((lane >> 4) & 1) * 2 + (lw & 1) * 4)) * 16); }
DI unsigned voff_n(int lane, unsigned ldb2) { return (unsigned)(lane >> 2) * ldb2 + (unsigned)(((lane & 3) ^ (((lane >> 5) & 1) * 2)) * 16); }
DI void issue_a(rsrc_t rA, lds_t st, int lw, unsigned voff, unsigned soff  , unsigned lda2) {
#pragma unroll
  for (int i = 0; i < 4; ++i) glds16(rA, st + (lw + 4 * i) * 1024, voff, soff + (unsigned)(lw + 4 * i) * 8u * lda2);
}
DI void issue_w(rsrc_t rB, lds_t blk, int lw, unsigned voff, unsigned soff  , unsigned ldb2) {
#pragma unroll
  for (int i = 0; i < 2; ++i) glds16(rB, blk + (lw + 4 * i) * 1024, voff, soff + (unsigned)(lw + 4 * i) * 8u * ldb2);
}
DI unsigned roff_w(int lane, int j) {
  const int g = lane >> 4, q = (lane >> 2) & 3, pp = lane & 3, f = ((q >> 1) & 1) | ((g & 1) << 1);
  return (unsigned)((8 * g + q) * 128 + pp * 8 + ((j ^ f) << 5));
}
DI unsigned roff_n(int lane, int j) {
  const int g = lane >> 4, q = (lane >> 2) & 3, pp = lane & 3;
  return (unsigned)(g * 512 + q * 64 + pp * 8 + ((j ^ (g & 1)) << 5));
}

__global__ __launch_bounds__(512) void k0(const u16* __restrict__ x, const u16* __restrict__ wqkv, const u16* __restrict__ w1,
                                          const u16* __restrict__ gw, char* __restrict__ ws) {
  __shared__ float red[8][64];
  STAMP0();
  const int w = threadIdx.x >> 6, lane = threadIdx.x & 63, b = blockIdx.x;
  const brs_t rW = mk_brs(ws, WS_BYTES);
  const int r = b * 8 + w;
  const u32x4 x0 = *(const u32x4*)(x + (size_t)r * D_ + lane * 8), x1 = *(const u32x4*)(x + (size_t)r * D_ + 512 + lane * 8);
  const u32x4 wv = *(const u32x4*)(w1 + (size_t)((b >> 4) * 64 + w * 8 + (lane >> 3)) * D_ + (b & 15) * 64 + (lane & 7) * 8);
  const u32x4 g0 = *(const u32x4*)(gw + lane * 8), g1 = *(const u32x4*)(gw + 512 + lane * 8);
  unsigned touch = 0;
  if (lane < 16) touch = *(const unsigned*)(wqkv + (size_t)(b * 4 + (w >> 1)) * 3072 + 1024 + ((w & 1) * 16 + lane) * 64);
  float s = 0.f, a = 0.f;
#pragma unroll
  for (int e = 0; e < 4; ++e) {
    s = dot2(x0[e], ONES, s); s = dot2(x1[e], ONES, s);
    a = dot2(cvt_pk(bf_lo(x0[e]) * bf_lo(g0[e]), bf_hi(x0[e]) * bf_hi(g0[e])), ONES, a);
    a = dot2(cvt_pk(bf_lo(x1[e]) * bf_lo(g1[e]), bf_hi(x1[e]) * bf_hi(g1[e])), ONES, a);
  }
  s = sum_r8(s); a = sum_r8(a);
#pragma unroll
  for (int o = 1; o < 8; o <<= 1) { s += __shfl_xor(s, o); a += __shfl_xor(a, o); }
  const float mean = s * (1.f / 1024.f);
  float q = 0.f;
#pragma unroll
  for (int e = 0; e < 4; ++e) {
    float d;
    d = bf_lo(x0[e]) - mean; q = __builtin_fmaf(d, d, q); d = bf_hi(x0[e]) - mean; q = __builtin_fmaf(d, d, q);
    d = bf_lo(x1[e]) - mean; q = __builtin_fmaf(d, d, q); d = bf_hi(x1[e]) - mean; q = __builtin_fmaf(d, d, q);
  }
  q = sum_r8(q);
#pragma unroll
  for (int o = 1; o < 8; o <<= 1) q += __shfl_xor(q, o);
  const float rstd = 1.f / sqrtf(q * (1.f / 1024.f) + 1e-5f);
  const float gg = rbf(1.f / (1.f + __expf(-rbf(a))));
  if (lane == 0) {
    u32x2 v; v.x = __float_as_uint(gg); v.y = __float_as_uint(rbf(1.f - gg)); st8(rW, (unsigned)WS_G + r * 8, v);
    v.x = __float_as_uint(rstd); v.y = __float_as_uint(-mean * rstd); st8(rW, (unsigned)WS_RS1 + r * 8, v);
  }
#pragma unroll
  for (int e = 0; e < 4; ++e) {
    const float lo = sum_r8(bf_lo(wv[e])), hi = sum_r8(bf_hi(wv[e]));
    if (lane < 8) { red[w][lane * 8 + e * 2] = lo; red[w][lane * 8 + e * 2 + 1] = hi; }
  }
  __syncthreads();
  if (w == 0) {
    float c = 0.f;
#pragma unroll
    for (int i = 0; i < 8; ++i) c += red[i][lane];
    __builtin_amdgcn_raw_buffer_store_b32(__float_as_uint(c), rW, (int)((unsigned)WS_CS1 + ((b >> 4) * 1024 + (b & 15) * 64 + lane) * 4), 0, 16);
    if (lane == 0) { u32x2 z = {0u, 0u}; st8(rW, (unsigned)WS_FLAG + b * 128, z); }
  }
  asm volatile("" ::"v"(touch));
  STAMP_END(4, 2);
}

typedef Cfg<3, 0, 6, 3> C1;
struct P1 {
  static constexpr int NBASE = 2;
  static constexpr bool trans(int n) { return n >= 4; }
  static constexpr bool narrow(int) { return false; }
  static constexpr int base(int i) { return i & 1; }
  static constexpr int imm(int i) { return (i >> 1) * 8192; }
};
struct L1 {
  static constexpr int NTAIL = 3;
  rsrc_t rA, rQ, rW, rS, rN; unsigned va, vq, vw, aoff, qcol, wcol; int lw, lane, pm, pn; lds_t xtra;
  DI void issue(lds_t st, int kt) {
    issue_a(rA, st, lw, va, aoff + (unsigned)kt * 128u, 2048u);
    issue_w(rQ, st + 16384, lw, vq, (unsigned)kt * (64u * 6144u) + qcol + 2048u, 6144u);
    issue_w(rQ, st + 16384 + 8192, lw, vq, (unsigned)kt * (64u * 6144u) + qcol + 4096u, 6144u);
    issue_w(rW, st + 16384 + 16384, lw, vw, (unsigned)kt * (64u * 2048u) + wcol, 2048u);
  }
  DI void tail(lds_t st) {
    glds16(rS, st + lw * 1024, (unsigned)((lw * 4 + (lane >> 4)) * 4096 + (lane & 15) * 16), (unsigned)WS_CS1 + pn * 256u);
    glds16(rS, xtra, (unsigned)lane * 16u, (unsigned)WS_RS1 + pm * 1024u);
    const unsigned line = (unsigned)(blockIdx.x * 64 + (lw & 1) * 32 + (lane & 31));
    gtouch((lw & 2) ? rN : rQ, xtra + 4096, (line >> 4) * ((lw & 2) ? 2048u : 6144u) + (line & 15) * 128u, 0u);
  }
};
__global__ __launch_bounds__(512, 2) void k1(const u16* __restrict__ x, const u16* __restrict__ wqkv, const u16* __restrict__ w1,
                                             const u16* __restrict__ w2, char* __restrict__ ws) {
  extern __shared__ __attribute__((aligned(16))) char smem_[];
  lds_t smem = (lds_t)smem_;
  typedef C1 C;
  STAMP0();
  const int w = __builtin_amdgcn_readfirstlane(threadIdx.x >> 6), lane = threadIdx.x & 63;
  const int xcd = blockIdx.x & 7, j = blockIdx.x >> 3;
  const int pm = (xcd & 1) * 8 + (j & 7), pn = (xcd >> 1) * 4 + (j >> 3);
  const int fi = lane & 15, g = lane >> 4;
  const brs_t rW = mk_brs(ws, WS_BYTES);
  LAS f32x2* xst = (LAS f32x2*)(smem + C::XTRA);
  LAS float* xcs = (LAS float*)(smem + C::XTRA + 1024);
  lds_t dump = smem + 2 * C::STAGE;
  if (w >= 4) {
    const int lw = w - 4;
    L1 L;
    L.rA = mk_rsrc(x, S_ * D_ * 2); L.rQ = mk_rsrc(wqkv, D_ * 3 * D_ * 2); L.rW = mk_rsrc(w1, D_ * D_ * 2); L.rS = mk_rsrc(ws, WS_BYTES); L.rN = mk_rsrc(w2, D_ * D_ * 2);
    L.lw = lw; L.lane = lane; L.pm = pm; L.pn = pn; L.xtra = smem + C::XTRA;
    L.va = voff_a(lane, lw, 2048u); L.vq = voff_w(lane, lw, 6144u); L.vw = voff_w(lane, lw, 2048u);
    L.aoff = (unsigned)pm * 128u * 2048u; L.qcol = (unsigned)pn * 128u; L.wcol = (unsigned)pn * 128u;
    produce<C>(smem, 16, L);
    wait_vm<1>();
    {
      f32x4 c = *(const LAS f32x4*)(smem + C::STAGE + lw * 1024 + lane * 16);
#pragma unroll
      for (int e = 0; e < 4; ++e) c[e] = sum_g(c[e]);
      if (g == 0) *(LAS f32x4*)(xcs + lw * 64 + fi * 4) = c;
    }
    wait_lgkm0();
    bar();
    const int wm = lw >> 1, mh = lw & 1;
    f32x4 cs[4];
#pragma unroll
    for (int n = 0; n < 4; ++n) {
      cs[n] = *(const LAS f32x4*)(xcs + n * 16 + g * 4);
#pragma unroll
      for (int i = 1; i < 4; ++i) cs[n] += *(const LAS f32x4*)(xcs + i * 64 + n * 16 + g * 4);
    }
    const int cg = pn >> 2, sh = (cg & 2) ? 2 : 1, dl = (cg & 1) ? -sh : sh;
    const unsigned hs = (unsigned)WS_HS + (pn * 64 + quad_col(lane)) * 2;
#pragma unroll
    for (int mm = 0; mm < 2; ++mm) {
      const int m = mh * 2 + mm, lr0 = wm * 64 + m * 16;
      const f32x2 rs = xst[lr0 + fi];
      unsigned p[4][2];
#pragma unroll
      for (int n = 0; n < 4; ++n) {
        const f32x4 a = *(const LAS f32x4*)(dump + (((wm * 2 + (n >> 1)) * 4 + m) * 2 + (n & 1)) * 1024 + lane * 16);
        float h[4];
#pragma unroll
        for (int r = 0; r < 4; ++r) h[r] = gelu_t(__builtin_fmaf(a[r], rs.x, rs.y * cs[n][r]));
        p[n][0] = cvt_pk(h[0], h[1]); p[n][1] = cvt_pk(h[2], h[3]);
      }
      u32x4 a = pair_swap(p[0][0], p[0][1], p[1][0], p[1][1]), b = pair_swap(p[2][0], p[2][1], p[3][0], p[3][1]);
      row_xchg(a, b);
      const int d0 = pm * 128 + lr0 + (fi & 7) + dl, d1 = d0 + 8;
      if (d0 >= 0) st16(rW, hs + d0 * 2048, a);
      if (d1 < S_) st16(rW, hs + d1 * 2048, b);
    }
    if (dl > 0 ? (pm == 0 && lw == 0) : (pm == 15 && lw == 3)) {
      if ((fi & 7) < sh) { const int zr = dl > 0 ? (fi & 7) : S_ - 1 - (fi & 7); const u32x4 zz = {0u, 0u, 0u, 0u}; st16(rW, hs + zr * 2048, zz); }
    }
    STAMP_END(0, 2);
    return;
  }
  const int wm = w >> 1, wn = w & 1;
  unsigned boff[2];
  boff[0] = 16384 + roff_w(lane, 2 * wn); boff[1] = 16384 + roff_w(lane, 2 * wn + 1);
  f32x4 acc[4][6];
  consume<C, P1>(smem, 16, acc, wm, lane, boff);
  STAMP(0, 1); STAMPC(0);
#pragma unroll
  for (int m = 0; m < 4; ++m)
#pragma unroll
    for (int n = 0; n < 2; ++n) *(LAS f32x4*)(dump + ((w * 4 + m) * 2 + n) * 1024 + lane * 16) = acc[m][4 + n];
  wait_lgkm0();
  bar();
  {
    const int head = pn * 2 + wn, part = pm * 2 + wm;
    f32x4 kv[2][2], z[2];
#pragma unroll
    for (int T = 0; T < 2; ++T) { z[T] = (f32x4){0.f, 0.f, 0.f, 0.f}; kv[T][0] = z[T]; kv[T][1] = z[T]; }
    const u32x4 onesv = {ONES, ONES, ONES, ONES};
#pragma unroll
    for (int mp = 0; mp < 2; ++mp) {
      u32x4 pk[2], vv[2];
#pragma unroll
      for (int T = 0; T < 2; ++T) {
        const f32x4 a = acc[2 * mp][T], b = acc[2 * mp + 1][T];
        pk[T].x = cvt_pk(phi_f(a[0]), phi_f(a[1])); pk[T].y = cvt_pk(phi_f(a[2]), phi_f(a[3]));
        pk[T].z = cvt_pk(phi_f(b[0]), phi_f(b[1])); pk[T].w = cvt_pk(phi_f(b[2]), phi_f(b[3]));
        const f32x4 c = acc[2 * mp][2 + T], d = acc[2 * mp + 1][2 + T];
        vv[T].x = cvt_pk(c[0], c[1]); vv[T].y = cvt_pk(c[2], c[3]); vv[T].z = cvt_pk(d[0], d[1]); vv[T].w = cvt_pk(d[2], d[3]);
      }
#pragma unroll
      for (int T = 0; T < 2; ++T) {
#pragma unroll
        for (int U = 0; U < 2; ++U)
          kv[T][U] = __builtin_amdgcn_mfma_f32_16x16x32_bf16(__builtin_bit_cast(bf16x8, pk[T]), __builtin_bit_cast(bf16x8, vv[U]), kv[T][U], 0, 0, 0);
        z[T] = __builtin_amdgcn_mfma_f32_16x16x32_bf16(__builtin_bit_cast(bf16x8, pk[T]), __builtin_bit_cast(bf16x8, onesv), z[T], 0, 0, 0);
      }
    }
    const unsigned kp = (unsigned)WS_KVP + (head * 32 + part) * 4096 + lane * 16;
#pragma unroll
    for (int T = 0; T < 2; ++T)
#pragma unroll
      for (int U = 0; U < 2; ++U) st16f(rW, kp + (T * 2 + U) * 1024, kv[T][U]);
    if (fi == 0) {
#pragma unroll
      for (int T = 0; T < 2; ++T) st16f(rW, (unsigned)WS_ZP + (head * 32 + part) * 128 + T * 64 + g * 16, z[T]);
    }
  }
  STAMP_END(0, 2);
}

typedef Cfg<2, 0, 4, 4> C2;
struct P2 {
  static constexpr int NBASE = 4;
  static constexpr bool trans(int) { return true; }
  static constexpr bool narrow(int) { return false; }
  static constexpr int base(int i) { return i; }
  static constexpr int imm(int) { return 0; }
};
struct L2 {
  static constexpr int NTAIL = 8;
  rsrc_t rA, rB, rN; unsigned va, vb, aoff, lda2, ldb2, bcol, noff; int lw, lane;
  DI void issue(lds_t st, int kt) {
    issue_a(rA, st, lw, va, aoff + (unsigned)kt * 128u, lda2);
    issue_w(rB, st + 16384, lw, vb, (unsigned)kt * 64u * ldb2 + bcol, ldb2);
    issue_w(rB, st + 16384 + 8192, lw, vb, (unsigned)kt * 64u * ldb2 + bcol + 128u, ldb2);
  }
  DI void tail(lds_t st) {
#pragma unroll
    for (int i = 0; i < 8; ++i) glds16(rN, st + (lw * 8 + i) * 1024, (unsigned)(lane >> 3) * 7168u + (lane & 7) * 16u, noff + i * (8u * 7168u));
  }
};
__global__ __launch_bounds__(512, 2) void k2(const u16* __restrict__ x, const u16* __restrict__ wqkv, const u16* __restrict__ w2,
                                             const u16* __restrict__ w3, char* __restrict__ ws) {
  extern __shared__ __attribute__((aligned(16))) char smem_[];
  lds_t smem = (lds_t)smem_;
  typedef C2 C;
  STAMP0();
  const int w = __builtin_amdgcn_readfirstlane(threadIdx.x >> 6), lane = threadIdx.x & 63;
  const int xcd = blockIdx.x & 7, j = blockIdx.x >> 3;
  const bool isq = xcd < 4;
  const int pm = (xcd & 1) * 8 + (j & 7), pn = ((xcd >> 1) & 1) * 4 + (j >> 3);
  const int fi = lane & 15, g = lane >> 4;
  const brs_t rW = mk_brs(ws, WS_BYTES);
  if (w >= 4) {
    const int lw = w - 4;
    L2 L;
    L.rA = mk_rsrc(isq ? (const void*)x : (const void*)(ws + WS_HS), S_ * D_ * 2);
    L.rB = isq ? mk_rsrc(wqkv, D_ * 3 * D_ * 2) : mk_rsrc(w2, D_ * D_ * 2);
    L.rN = mk_rsrc(w3, D_ * F_ * 2);
    L.lw = lw; L.lane = lane; L.lda2 = 2048u; L.ldb2 = isq ? 6144u : 2048u;
    L.va = voff_a(lane, lw, 2048u); L.vb = voff_w(lane, lw, L.ldb2);
    L.aoff = (unsigned)pm * 128u * 2048u; L.bcol = (unsigned)pn * 256u;
    const int u = blockIdx.x < 224 ? blockIdx.x : blockIdx.x - 224, slab = u % 56, rg = (u / 56) * 4 + lw;
    L.noff = (unsigned)rg * (64u * 7168u) + slab * 128u;
    produce<C>(smem, 16, L);
    wait_vm<0>();
    float c[8];
#pragma unroll
    for (int e = 0; e < 8; ++e) c[e] = 0.f;
#pragma unroll
    for (int i = 0; i < 8; ++i) {
      const u32x4 v = *(const LAS u32x4*)(smem + (lw * 8 + i) * 1024 + lane * 16);
#pragma unroll
      for (int e = 0; e < 4; ++e) { c[2 * e] += bf_lo(v[e]); c[2 * e + 1] += bf_hi(v[e]); }
    }
#pragma unroll
    for (int e = 0; e < 8; ++e) c[e] = sum_r8(c[e]);
    if (lane < 8 && blockIdx.x < 224) {
      const unsigned o = (unsigned)WS_CS3 + (rg * F_ + slab * 64 + lane * 8) * 4;
      st16f(rW, o, (f32x4){c[0], c[1], c[2], c[3]}); st16f(rW, o + 16, (f32x4){c[4], c[5], c[6], c[7]});
    }
    STAMP_END(1, 2);
    return;
  }
  const int wm = w >> 1, wn = w & 1;
  const unsigned eoff = (unsigned)((pm * 128 + wm * 64 + (fi & 7)) * D_ + pn * 128 + wn * 64 + quad_col(lane)) * 2;
  u32x4 xr[4][2];
  if (!isq) {
#pragma unroll
    for (int m = 0; m < 4; ++m)
#pragma unroll
      for (int p = 0; p < 2; ++p) xr[m][p] = *(const u32x4*)((const char*)x + eoff + (m * 16 + p * 8) * 2048);
  }
  const int kh = blockIdx.x >> 3, kTU = (blockIdx.x >> 1) & 3, khalf = blockIdx.x & 1;
  LAS f32x4* red = (LAS f32x4*)(smem + C::XTRA);
  {
    const char* p = ws + WS_KVP + (size_t)(((kh * 32 + w * 8 + (lane >> 5)) * 4 + kTU) * 1024 + (khalf * 32 + (lane & 31)) * 16);
    f32x4 s = *(const f32x4*)p;
#pragma unroll
    for (int i = 1; i < 4; ++i) s += *(const f32x4*)(p + i * 8192);
    red[w * 64 + lane] = s;
    if (w == 3 && (blockIdx.x & 7) == 0) {
      const char* zp = ws + WS_ZP + (size_t)kh * 4096 + lane * 16;
      f32x4 z = *(const f32x4*)zp;
#pragma unroll
      for (int i = 1; i < 4; ++i) z += *(const f32x4*)(zp + i * 1024);
#pragma unroll
      for (int e = 0; e < 4; ++e) z[e] = sum_r8(z[e]);
      u32x2 v; v.x = cvt_pk(z[0], z[1]); v.y = cvt_pk(z[2], z[3]);
      if (lane < 8) st8(rW, (unsigned)WS_ZF + kh * 64 + lane * 8, v);
    }
  }
  unsigned boff[4];
#pragma unroll
  for (int i = 0; i < 4; ++i) boff[i] = 16384 + wn * 8192 + roff_w(lane, i);
  f32x4 acc[4][4];
  consume<C, P2>(smem, 16, acc, wm, lane, boff);
  STAMP(1, 1); STAMPC(1);
  if (isq) {
#pragma unroll
    for (int m = 0; m < 4; ++m) {
      unsigned p[4][2];
#pragma unroll
      for (int n = 0; n < 4; ++n) { p[n][0] = cvt_pk(phi_f(acc[m][n][0]), phi_f(acc[m][n][1])); p[n][1] = cvt_pk(phi_f(acc[m][n][2]), phi_f(acc[m][n][3])); }
      store_quad(rW, (unsigned)WS_PQ + eoff + m * 32768, 16384, p);
    }
  } else {
#pragma unroll
    for (int m = 0; m < 4; ++m) {
      float s1 = 0.f, s2 = 0.f;
      unsigned p[4][2];
      unpack_quad(xr[m][0], xr[m][1], p);
#pragma unroll
      for (int n = 0; n < 4; ++n) {
        p[n][0] = cvt_pk(acc[m][n][0] + bf_lo(p[n][0]), acc[m][n][1] + bf_hi(p[n][0]));
        p[n][1] = cvt_pk(acc[m][n][2] + bf_lo(p[n][1]), acc[m][n][3] + bf_hi(p[n][1]));
        s1 = dot2(p[n][0], ONES, s1); s1 = dot2(p[n][1], ONES, s1);
        s2 = dot2(p[n][0], p[n][0], s2); s2 = dot2(p[n][1], p[n][1], s2);
      }
      store_quad(rW, (unsigned)WS_X1 + eoff + m * 32768, 16384, p);
      s1 = sum_g(s1); s2 = sum_g(s2);
      if (g == 0) { u32x2 v; v.x = __float_as_uint(s1); v.y = __float_as_uint(s2); st8(rW, (unsigned)WS_ST + (pm * 128 + wm * 64 + m * 16 + fi) * 128 + (pn * 2 + wn) * 8, v); }
    }
  }
  if (w == 0 && lane < 32) {
    f32x4 s = red[lane];
#pragma unroll
    for (int i = 1; i < 8; ++i) s += red[i * 32 + lane];
    const int l = khalf * 32 + lane, gg = l >> 4, T = kTU >> 1, U = kTU & 1;
    u32x2 v; v.x = cvt_pk(s[0], s[1]); v.y = cvt_pk(s[2], s[3]);
    st8(rW, (unsigned)WS_KVF + (kh * 2 + U) * 1024 + ((2 * T + (gg >> 1)) * 16 + (l & 15)) * 16 + (gg & 1) * 8, v);
  }
  STAMP_END(1, 2);
}

typedef Cfg<3, 1, 7, 3> C3;
struct P3 {
  static constexpr int NBASE = 7;
  static constexpr bool trans(int) { return true; }
  static constexpr bool narrow(int i) { return i == 6; }
  static constexpr int base(int i) { return i; }
  static constexpr int imm(int) { return 0; }
};
struct L3 {
  static constexpr int NTAIL = 11;
  rsrc_t rA, rB, rS, rO, rF; unsigned va, vb, vn, aoff, wcol, ncol; int lw, lane, pm, pn; lds_t xtra;
  DI void issue(lds_t st, int kt) {
    issue_a(rA, st, lw, va, aoff + (unsigned)kt * 128u, 2048u);
    const unsigned kb = (unsigned)kt * (64u * 7168u);
#pragma unroll
    for (int b = 0; b < 3; ++b) issue_w(rB, st + 16384 + b * 8192, lw, vb, kb + wcol + b * 128u, 7168u);
    glds16(rB, st + 16384 + 24576 + lw * 1024, vn, kb + ncol + (unsigned)lw * (16u * 7168u));
  }
  DI void tail(lds_t st) {
    st += lw * 9216;
    const unsigned l16 = (unsigned)lane * 16u;
#pragma unroll
    for (int i = 0; i < 4; ++i) glds16(rS, st + i * 1024, l16, (unsigned)WS_ST + (pm * 128 + lw * 32) * 128u + i * 1024u);
#pragma unroll
    for (int i = 0; i < 4; ++i) glds16(rS, st + 4096 + i * 1024, l16, (unsigned)WS_CS3 + ((lw * 4 + i) * F_ + pn * 224) * 4u);
    glds16(rS, st + 8192, l16, (unsigned)WS_G + pm * 1024u);
    const unsigned lo = (unsigned)((blockIdx.x * 4 + lw) * 16 + (lane & 15)), lf = (unsigned)((blockIdx.x * 4 + lw) * 56 + (lane < 56 ? lane : 55));
    gtouch(rF, xtra + 6144, lf * 128u, 0u);
    gtouch(rO, xtra + 6144, lo * 128u, 0u);
  }
};
__global__ __launch_bounds__(512, 2) void k3(const u16* __restrict__ w3, const u16* __restrict__ wo, const u16* __restrict__ w4, char* __restrict__ ws) {
  extern __shared__ __attribute__((aligned(16))) char smem_[];
  lds_t smem = (lds_t)smem_;
  typedef C3 C;
  STAMP0();
  const int w = __builtin_amdgcn_readfirstlane(threadIdx.x >> 6), lane = threadIdx.x & 63;
  const int xcd = blockIdx.x & 7, j = blockIdx.x >> 3;
  const int pm = (xcd & 1) * 8 + (j & 7), pn = (xcd >> 1) * 4 + (j >> 3);
  const int fi = lane & 15, g = lane >> 4;
  const brs_t rW = mk_brs(ws, WS_BYTES);
  const int wc0 = (pn & 1) ? 32 : 0, nc0 = (pn & 1) ? 0 : 192;
  LAS f32x4* rst = (LAS f32x4*)(smem + C::XTRA);
  LAS float* xcs = (LAS float*)(smem + C::XTRA + 2048);
  lds_t dump = smem + C::STAGE + 36864;
  const unsigned urow = (unsigned)WS_AU + ((pm * 128) * LDAU + 1024 + pn * 224) * 2;
  if (w >= 4) {
    const int lw = w - 4;
    L3 L;
    L.rA = mk_rsrc(ws + WS_X1, S_ * D_ * 2); L.rB = mk_rsrc(w3, D_ * F_ * 2); L.rS = mk_rsrc(ws, WS_BYTES);
    L.rO = mk_rsrc(wo, D_ * D_ * 2); L.rF = mk_rsrc(w4, F_ * D_ * 2);
    L.lw = lw; L.lane = lane; L.pm = pm; L.pn = pn; L.xtra = smem + C::XTRA;
    L.va = voff_a(lane, lw, 2048u); L.vb = voff_w(lane, lw, 7168u); L.vn = voff_n(lane, 7168u);
    L.aoff = (unsigned)pm * 128u * 2048u; L.wcol = (unsigned)(pn * 224 + wc0) * 2u; L.ncol = (unsigned)(pn * 224 + nc0) * 2u;
    produce<C>(smem, 16, L);
    wait_vm<2>();
    {
      lds_t st = smem + C::STAGE + lw * 9216;
      f32x4 s = *(const LAS f32x4*)(st + lane * 64), c = *(const LAS f32x4*)(st + 4096 + lane * 16);
#pragma unroll
      for (int i = 1; i < 4; ++i) { s += *(const LAS f32x4*)(st + lane * 64 + i * 16); c += *(const LAS f32x4*)(st + 4096 + i * 1024 + lane * 16); }
      const int lr = lw * 32 + (lane >> 1);
      const float omg = *(const LAS float*)(st + 8192 + lr * 8 + 4);
      float s1 = s[0] + s[2], s2 = s[1] + s[3];
      s1 += __shfl_xor(s1, 1); s2 += __shfl_xor(s2, 1);
      const float mean = s1 * (1.f / 1024.f);
      const float var = fmaxf(s2 * (1.f / 1024.f) - mean * mean, 0.f);
      const float rstd = 1.f / sqrtf(var + 1e-5f);
      if (!(lane & 1)) rst[lr] = (f32x4){rstd, -mean * rstd, omg, 0.f};
      *(LAS f32x4*)(xcs + lw * 256 + lane * 4) = c;
    }
    wait_lgkm0();
    bar();
    const int wm = lw >> 1, mh = lw & 1;
    f32x4 cs[6];
#pragma unroll
    for (int n = 0; n < 6; ++n) {
      const int c0 = (n < 4 ? wc0 + 128 + n * 16 : nc0 + (n - 4) * 16) + g * 4;
      cs[n] = *(const LAS f32x4*)(xcs + c0);
#pragma unroll
      for (int i = 1; i < 4; ++i) cs[n] += *(const LAS f32x4*)(xcs + i * 256 + c0);
    }
#pragma unroll
    for (int mm = 0; mm < 2; ++mm) {
      const int m = mh * 2 + mm, lr0 = wm * 64 + m * 16;
      const f32x4 rs = rst[lr0 + fi];
      unsigned p[4][2], q[2][2];
#pragma unroll
      for (int n = 0; n < 6; ++n) {
        const int sw = n < 4 ? (n >> 1) : (n - 4), si = n < 4 ? (n & 1) : 2;
        const f32x4 a = *(const LAS f32x4*)(dump + (((wm * 2 + sw) * 4 + m) * 3 + si) * 1024 + lane * 16);
        float h[4];
#pragma unroll
        for (int r = 0; r < 4; ++r) h[r] = rs[2] * gelu_t(__builtin_fmaf(a[r], rs[0], rs[1] * cs[n][r]));
        if (n < 4) { p[n][0] = cvt_pk(h[0], h[1]); p[n][1] = cvt_pk(h[2], h[3]); }
        else { q[n - 4][0] = cvt_pk(h[0], h[1]); q[n - 4][1] = cvt_pk(h[2], h[3]); }
      }
      store_quad(rW, urow + ((lr0 + (fi & 7)) * LDAU + wc0 + 128 + quad_col(lane)) * 2, 8 * LDAU * 2, p);
      st16(rW, urow + ((lr0 + fi) * LDAU + nc0 + (g & 1) * 16 + (g & 2) * 4) * 2, pair_swap(q[0][0], q[0][1], q[1][0], q[1][1]));
    }
    STAMP_END(2, 2);
    return;
  }
  const int wm = w >> 1, wn = w & 1;
  {
    const int head = pn * 2 + wn, r0 = pm * 128 + wm * 64 + fi;
    const bf16x8 kf0 = *(const bf16x8*)(ws + WS_KVF + (size_t)(head * 2) * 1024 + lane * 16);
    const bf16x8 kf1 = *(const bf16x8*)(ws + WS_KVF + (size_t)(head * 2 + 1) * 1024 + lane * 16);
    const bf16x8 zf = *(const bf16x8*)(ws + WS_ZF + (size_t)head * 64 + g * 16);
    bf16x8 pq[4]; float gg[4];
#pragma unroll
    for (int m = 0; m < 4; ++m) {
      pq[m] = *(const bf16x8*)((const u16*)(ws + WS_PQ) + (size_t)(r0 + m * 16) * D_ + head * 32 + g * 8);
      gg[m] = *(const float*)(ws + WS_G + (size_t)(r0 + m * 16) * 8);
    }
#pragma unroll
    for (int m = 0; m < 4; ++m) {
      const f32x4 zero = {0.f, 0.f, 0.f, 0.f};
      const f32x4 n0 = __builtin_amdgcn_mfma_f32_16x16x32_bf16(kf0, pq[m], zero, 0, 0, 0);
      const f32x4 n1 = __builtin_amdgcn_mfma_f32_16x16x32_bf16(kf1, pq[m], zero, 0, 0, 0);
      const f32x4 dn = __builtin_amdgcn_mfma_f32_16x16x32_bf16(zf, pq[m], zero, 0, 0, 0);
      const float sc = gg[m] / (dn[0] + 1e-6f);
      const u32x4 v = pair_swap(cvt_pk(n0[0] * sc, n0[1] * sc), cvt_pk(n0[2] * sc, n0[3] * sc), cvt_pk(n1[0] * sc, n1[1] * sc), cvt_pk(n1[2] * sc, n1[3] * sc));
      st16(rW, (unsigned)WS_AU + ((r0 + m * 16) * LDAU + head * 32 + (g & 1) * 16 + (g & 2) * 4) * 2, v);
    }
  }
  unsigned boff[7];
#pragma unroll
  for (int i = 0; i < 4; ++i) boff[i] = 16384 + wn * 8192 + roff_w(lane, i);
  boff[4] = 16384 + 16384 + roff_w(lane, 2 * wn); boff[5] = 16384 + 16384 + roff_w(lane, 2 * wn + 1);
  boff[6] = 16384 + 24576 + roff_n(lane, wn);
  f32x4 acc[4][7];
  consume<C, P3>(smem, 16, acc, wm, lane, boff);
  STAMP(2, 1); STAMPC(2);
#pragma unroll
  for (int m = 0; m < 4; ++m)
#pragma unroll
    for (int n = 0; n < 3; ++n) *(LAS f32x4*)(dump + ((w * 4 + m) * 3 + n) * 1024 + lane * 16) = acc[m][4 + n];
  wait_lgkm0();
  bar();
  f32x4 cs[4];
#pragma unroll
  for (int n = 0; n < 4; ++n) {
    const int c0 = wc0 + wn * 64 + n * 16 + g * 4;
    cs[n] = *(const LAS f32x4*)(xcs + c0);
#pragma unroll
    for (int i = 1; i < 4; ++i) cs[n] += *(const LAS f32x4*)(xcs + i * 256 + c0);
  }
#pragma unroll
  for (int m = 0; m < 4; ++m) {
    const int lr0 = wm * 64 + m * 16;
    const f32x4 rs = rst[lr0 + fi];
    unsigned p[4][2];
#pragma unroll
    for (int n = 0; n < 4; ++n) {
      float h[4];
#pragma unroll
      for (int r = 0; r < 4; ++r) h[r] = rs[2] * gelu_t(__builtin_fmaf(acc[m][n][r], rs[0], rs[1] * cs[n][r]));
      p[n][0] = cvt_pk(h[0], h[1]); p[n][1] = cvt_pk(h[2], h[3]);
    }
    store_quad(rW, urow + ((lr0 + (fi & 7)) * LDAU + wc0 + wn * 64 + quad_col(lane)) * 2, 8 * LDAU * 2, p);
  }
  STAMP_END(2, 2);
}

typedef Cfg<2, 0, 4, 4> C4;
struct L4 {
  static constexpr int NTAIL = 0;
  rsrc_t rA, rO, rW; unsigned va, vb, aoff, bcol; int lw, kt0;
  DI void issue(lds_t st, int t) {
    const int kt = kt0 + t;
    issue_a(rA, st, lw, va, aoff + (unsigned)kt * 128u, LDAU * 2u);
    const bool o = kt < 16;
    rsrc_t rB; rB.x = o ? rO.x : rW.x; rB.y = o ? rO.y : rW.y; rB.z = o ? rO.z : rW.z; rB.w = rO.w;
    const unsigned kb = (unsigned)(o ? kt : kt - 16) * (64u * 2048u) + bcol;
    issue_w(rB, st + 16384, lw, vb, kb, 2048u);
    issue_w(rB, st + 16384 + 8192, lw, vb, kb + 128u, 2048u);
  }
  DI void tail(lds_t) {}
};
__global__ __launch_bounds__(512, 2) void k4(const u16* __restrict__ wo, const u16* __restrict__ w4, char* __restrict__ ws, u16* __restrict__ out) {
  extern __shared__ __attribute__((aligned(16))) char smem_[];
  lds_t smem = (lds_t)smem_;
  typedef C4 C;
  STAMP0();
  const int w = __builtin_amdgcn_readfirstlane(threadIdx.x >> 6), lane = threadIdx.x & 63;
  const int xcd = blockIdx.x & 7, j = blockIdx.x >> 3;
  const int hf = xcd >> 2;
  const int pm = (xcd & 1) * 8 + (j & 7), pn = ((xcd >> 1) & 1) * 4 + (j >> 3);
  const int fi = lane & 15;
  const brs_t rW = mk_brs(ws, WS_BYTES);
  if (w >= 4) {
    const int lw = w - 4;
    L4 L;
    L.rA = mk_rsrc(ws + WS_AU, S_ * LDAU * 2); L.rO = mk_rsrc(wo, D_ * D_ * 2); L.rW = mk_rsrc(w4, F_ * D_ * 2);
    L.lw = lw; L.kt0 = hf * 36; L.va = voff_a(lane, lw, LDAU * 2u); L.vb = voff_w(lane, lw, 2048u);
    L.aoff = (unsigned)pm * 128u * (LDAU * 2u); L.bcol = (unsigned)pn * 256u;
    produce<C>(smem, 36, L);
    STAMP_END(3, 2);
    return;
  }
  const int wm = w >> 1, wn = w & 1;
  const int row0 = pm * 128 + wm * 64 + hf * 32;
  const unsigned eoff = (unsigned)((row0 + (fi & 7)) * D_ + pn * 128 + wn * 64 + quad_col(lane)) * 2;
  u32x4 xr[2][2]; float omg[2];
#pragma unroll
  for (int m = 0; m < 2; ++m) {
#pragma unroll
    for (int p = 0; p < 2; ++p) xr[m][p] = *(const u32x4*)(ws + WS_X1 + eoff + (m * 16 + p * 8) * 2048);
    omg[m] = *(const float*)(ws + WS_G + (size_t)(row0 + m * 16 + fi) * 8 + 4);
  }
  unsigned boff[4];
#pragma unroll
  for (int i = 0; i < 4; ++i) boff[i] = 16384 + wn * 8192 + roff_w(lane, i);
  f32x4 acc[4][4];
  consume<C, P2>(smem, 36, acc, wm, lane, boff);
  STAMP(3, 1); STAMPC(3);
  f32x4 own[2][4];
  {
    const unsigned ob = (unsigned)WS_XCH + (blockIdx.x * 4 + w) * 8192 + lane * 16;
#pragma unroll
    for (int m = 0; m < 2; ++m)
#pragma unroll
      for (int n = 0; n < 4; ++n) {
        f32x4 snd;
#pragma unroll
        for (int r = 0; r < 4; ++r) { snd[r] = hf ? acc[m][n][r] : acc[2 + m][n][r]; own[m][n][r] = hf ? acc[2 + m][n][r] : acc[m][n][r]; }
        st16f(rW, ob + (m * 4 + n) * 1024, snd);
      }
    wait_vm<0>();
    STAMP(3, 3);
    bar();
    if (threadIdx.x == 0) __builtin_amdgcn_raw_buffer_store_b32(1u, rW, (int)((unsigned)WS_FLAG + blockIdx.x * 128), 0, 16);
  }
  {
    const char* fp = ws + WS_FLAG + (size_t)(blockIdx.x ^ 4) * 128;
    unsigned v = 0;
    for (int it = 0; it < (1 << 18); ++it) {
      asm volatile("global_load_dword %0, %1, off sc1\n\ts_waitcnt vmcnt(0)" : "=v"(v) : "v"(fp) : "memory");
      if (__builtin_amdgcn_readfirstlane(v) != 0u) break;
      __builtin_amdgcn_s_sleep(1);
    }
    STAMP(3, 4);
    const unsigned ib = (unsigned)WS_XCH + ((blockIdx.x ^ 4) * 4 + w) * 8192 + lane * 16;
    f32x4 o[2][4];
#pragma unroll
    for (int m = 0; m < 2; ++m)
#pragma unroll
      for (int n = 0; n < 4; ++n) o[m][n] = __builtin_bit_cast(f32x4, __builtin_amdgcn_raw_buffer_load_b128(rW, (int)(ib + (m * 4 + n) * 1024), 0, 16));
    const brs_t rO = mk_brs(out, S_ * D_ * 2);
#pragma unroll
    for (int m = 0; m < 2; ++m) {
      unsigned p[4][2];
      unpack_quad(xr[m][0], xr[m][1], p);
      const float q = omg[m];
#pragma unroll
      for (int n = 0; n < 4; ++n) {
        const f32x4 a = own[m][n] + o[m][n];
        p[n][0] = cvt_pk(__builtin_fmaf(q, bf_lo(p[n][0]), a[0]), __builtin_fmaf(q, bf_hi(p[n][0]), a[1]));
        p[n][1] = cvt_pk(__builtin_fmaf(q, bf_lo(p[n][1]), a[2]), __builtin_fmaf(q, bf_hi(p[n][1]), a[3]));
      }
      store_quad(rO, eoff + m * 32768, 16384, p);
    }
  }
  STAMP_END(3, 2);
}

extern "C" __global__ void kend() {}

extern "C" void kernel_launch(void* const* d_in, const int* in_sizes, int n_in, void* d_out, int out_size, void* d_ws, size_t ws_size,
                              hipStream_t stream) {
  const u16* x = (const u16*)d_in[0];
  const u16* wqkv = (const u16*)d_in[1];
  const u16* wo = (const u16*)d_in[2];
  const u16* w1 = (const u16*)d_in[4];
  const u16* w2 = (const u16*)d_in[5];
  const u16* w3 = (const u16*)d_in[6];
  const u16* w4 = (const u16*)d_in[7];
  const u16* gw = (const u16*)d_in[8];
  char* ws = (char*)d_ws;
  (void)hipFuncSetAttribute((const void*)k1, hipFuncAttributeMaxDynamicSharedMemorySize, C1::LDS);
  (void)hipFuncSetAttribute((const void*)k2, hipFuncAttributeMaxDynamicSharedMemorySize, C2::LDS);
  (void)hipFuncSetAttribute((const void*)k3, hipFuncAttributeMaxDynamicSharedMemorySize, C3::LDS);
  (void)hipFuncSetAttribute((const void*)k4, hipFuncAttributeMaxDynamicSharedMemorySize, C4::LDS);
  k0<<<256, 512, 0, stream>>>(x, wqkv, w1, gw, ws);
  k1<<<256, 512, C1::LDS, stream>>>(x, wqkv, w1, w2, ws);
  k2<<<256, 512, C2::LDS, stream>>>(x, wqkv, w2, w3, ws);
  k3<<<256, 512, C3::LDS, stream>>>(w3, wo, w4, ws);
  k4<<<256, 512, C4::LDS, stream>>>(wo, w4, ws, (u16*)d_out);
}
```

```cpp
#include <hip/hip_runtime.h>

typedef unsigned short u16;
typedef unsigned long long u64;
typedef short bf16x8 __attribute__((ext_vector_type(8)));
typedef short s16x4 __attribute__((ext_vector_type(4)));
typedef float f32x4 __attribute__((ext_vector_type(4)));
typedef float f32x2 __attribute__((ext_vector_type(2)));
typedef unsigned u32x4 __attribute__((ext_vector_type(4)));
typedef unsigned u32x2 __attribute__((ext_vector_type(2)));
typedef __bf16 bf16x2_t __attribute__((ext_vector_type(2)));
#define LAS __attribute__((address_space(3)))
typedef LAS char* lds_t;
typedef u32x4 rsrc_t;
#define DI __device__ __forceinline__

constexpr int S_ = 2048, D_ = 1024, F_ = 3584;
constexpr int LDAU = 4608 + 64;
constexpr size_t MB = 1u << 20;
constexpr size_t WS_HS = 0, WS_PQ = 4 * MB, WS_X1 = 8 * MB, WS_AU = 12 * MB, WS_KVP = 32 * MB, WS_ZP = 36 * MB,
                 WS_KVF = 36 * MB + 512 * 1024, WS_ZF = 36 * MB + 768 * 1024, WS_ST = 37 * MB, WS_G = 37 * MB + 512 * 1024,
                 WS_RS1 = 37 * MB + 640 * 1024, WS_CS1 = 37 * MB + 768 * 1024, WS_FLAG = 38 * MB, WS_CS3 = 39 * MB, WS_XCH = 40 * MB;

DI rsrc_t mk_rsrc(const void* p, unsigned bytes) {
  u64 a = (u64)p; rsrc_t r; r.x = (unsigned)a; r.y = (unsigned)(a >> 32) & 0xffffu; r.z = bytes; r.w = 0x00020000u; return r;
}
DI void glds16(rsrc_t r, lds_t dst, unsigned voff, unsigned soff) {
  asm volatile("s_mov_b32 m0, %0\n\ts_nop 0\n\tbuffer_load_dwordx4 %1, %2, %3 offen lds" ::"s"((unsigned)(size_t)dst), "v"(voff), "s"(r), "s"(soff) : "memory");
}
DI void gtouch(rsrc_t r, lds_t dst, unsigned voff, unsigned soff) {
  asm volatile("s_mov_b32 m0, %0\n\ts_nop 0\n\tbuffer_load_dword %1, %2, %3 offen lds" ::"s"((unsigned)(size_t)dst), "v"(voff), "s"(r), "s"(soff) : "memory");
}
DI void glds16c(rsrc_t r, lds_t dst, unsigned voff, unsigned soff) {
  asm volatile("s_mov_b32 m0, %0\n\ts_nop 0\n\tbuffer_load_dwordx4 %1, %2, %3 offen sc1 lds" ::"s"((unsigned)(size_t)dst), "v"(voff), "s"(r), "s"(soff) : "memory");
}
DI void gpoll(rsrc_t r, lds_t dst, unsigned voff) {
  asm volatile("s_mov_b32 m0, %0\n\ts_nop 0\n\tbuffer_load_dword %1, %2, 0 offen sc1 lds" ::"s"((unsigned)(size_t)dst), "v"(voff), "s"(r) : "memory");
}
template <int N> DI void wait_vm() { asm volatile("s_waitcnt vmcnt(%0)" ::"n"(N) : "memory"); }
DI void wait_lgkm0() { asm volatile("" ::: "memory"); __builtin_amdgcn_s_waitcnt(0xC07F); asm volatile("" ::: "memory"); }
DI void bar() { asm volatile("" ::: "memory"); __builtin_amdgcn_s_barrier(); asm volatile("" ::: "memory"); }
DI unsigned cvt_pk(float lo, float hi) { f32x2 v = {lo, hi}; bf16x2_t r = __builtin_convertvector(v, bf16x2_t); return __builtin_bit_cast(unsigned, r); }
DI float bf_lo(unsigned u) { return __builtin_bit_cast(float, u << 16); }
DI float bf_hi(unsigned u) { return __builtin_bit_cast(float, u & 0xffff0000u); }
DI float rbf(float f) { return bf_lo(cvt_pk(f, 0.f) & 0xffffu); }
DI float dot2(unsigned a, unsigned b, float c) { return __builtin_amdgcn_fdot2_f32_bf16(__builtin_bit_cast(bf16x2_t, a), __builtin_bit_cast(bf16x2_t, b), c, false); }
constexpr unsigned ONES = 0x3f803f80u;
typedef __amdgpu_buffer_rsrc_t brs_t;
constexpr unsigned WS_BYTES = 48u << 20;
DI brs_t mk_brs(void* p, unsigned bytes) { return __builtin_amdgcn_make_buffer_rsrc(p, (short)0, (int)bytes, 0x00020000); }
#define CT 0
#define WSOFF 0
#define STOFF (WSOFF ? -(long)(WSOFF) : (long)(64 * MB))
#define SAUX 16
DI void st16(brs_t r, unsigned off, u32x4 v) { __builtin_amdgcn_raw_buffer_store_b128(v, r, (int)off, 0, SAUX); }
DI void st8(brs_t r, unsigned off, u32x2 v) { __builtin_amdgcn_raw_buffer_store_b64(v, r, (int)off, 0, SAUX); }
DI void st16f(brs_t r, unsigned off, f32x4 v) { __builtin_amdgcn_raw_buffer_store_b128(__builtin_bit_cast(u32x4, v), r, (int)off, 0, 16); }
#define STAMP(kid, i) do {} while (0)
#define STAMP_END(kid, i) do {} while (0)
#define CARG(k)
#define LST(v) do {} while (0)
#define LST_OUT(kid) do {} while (0)
#define LST_OUT2(kid, L) do {} while (0)
#define STAMP_NW(kid, i) do {} while (0)
#define STAMP0() do {} while (0)
#define STAMPC(kid) do {} while (0)
#define CODE_TOUCH(E, i) do { u64 pc_; unsigned len_; asm volatile("s_getpc_b64 %0\n1:\n\ts_mov_b32 %1, " E "-1b" : "=s"(pc_), "=s"(len_)); \
    const unsigned o_ = (unsigned)(i) * 128u; if (o_ < len_) { unsigned t_; asm volatile("global_load_dword %0, %1, %2" : "=v"(t_) : "v"(o_), "s"(pc_) : "memory"); ct_ = t_; } } while (0)
DI float sum_g(float v) {
  unsigned u = __builtin_bit_cast(unsigned, v);
  auto a = __builtin_amdgcn_permlane16_swap(u, u, false, false);
  const unsigned a0 = a[0], a1 = a[1];
  v = __uint_as_float(a0) + __uint_as_float(a1);
  u = __builtin_bit_cast(unsigned, v);
  auto b = __builtin_amdgcn_permlane32_swap(u, u, false, false);
  const unsigned b0 = b[0], b1 = b[1];
  return __uint_as_float(b0) + __uint_as_float(b1);
}
DI float gelu_t(float v) {
  const float e = __builtin_amdgcn_exp2f(v * __builtin_fmaf(v * v, -0.10294323970f, -2.30220819814f));
  return v * __builtin_amdgcn_rcpf(1.f + e);
}
DI float phi_f(float t) { return (t > 0.f ? t + 1.f : __builtin_amdgcn_exp2f(t * 1.44269504089f)) + 1e-6f; }
DI void swap16(unsigned& x, unsigned& y) { auto r = __builtin_amdgcn_permlane16_swap(x, y, false, false); const unsigned r0 = r[0], r1 = r[1]; x = r0; y = r1; }
DI void swap32(unsigned& x, unsigned& y) { auto r = __builtin_amdgcn_permlane32_swap(x, y, false, false); const unsigned r0 = r[0], r1 = r[1]; x = r0; y = r1; }
DI void tile_xpose(unsigned (&p)[4][2]) {
#pragma unroll
  for (int e = 0; e < 2; ++e) { swap32(p[0][e], p[2][e]); swap32(p[1][e], p[3][e]); swap16(p[0][e], p[1][e]); swap16(p[2][e], p[3][e]); }
}
DI void par_xchg(u32x4& a, u32x4& b, bool odd) {
#pragma unroll
  for (int e = 0; e < 4; ++e) {
    const unsigned x = a[e], y = b[e];
    const unsigned xs = (unsigned)__builtin_amdgcn_mov_dpp((int)x, 0xb1, 0xf, 0xf, true), ys = (unsigned)__builtin_amdgcn_mov_dpp((int)y, 0xb1, 0xf, 0xf, true);
    a[e] = odd ? ys : x; b[e] = odd ? y : xs;
  }
}
DI void pack_quad(unsigned (&p)[4][2], u32x4& a, u32x4& b, bool odd) {
  tile_xpose(p);
  a.x = p[0][0]; a.y = p[0][1]; a.z = p[1][0]; a.w = p[1][1]; b.x = p[2][0]; b.y = p[2][1]; b.z = p[3][0]; b.w = p[3][1];
  par_xchg(a, b, odd);
}
DI void store_quad(brs_t r, unsigned off  , unsigned pitch, unsigned (&p)[4][2], bool odd) {
  u32x4 a, b;
  pack_quad(p, a, b, odd);
  st16(r, off, a); st16(r, off + pitch, b);
}
DI void unpack_quad(u32x4 a, u32x4 b, unsigned (&p)[4][2], bool odd) {
  par_xchg(a, b, odd);
  p[0][0] = a.x; p[0][1] = a.y; p[1][0] = a.z; p[1][1] = a.w; p[2][0] = b.x; p[2][1] = b.y; p[3][0] = b.z; p[3][1] = b.w;
  tile_xpose(p);
}
DI float sum_r8(float v) {
  v += __int_as_float(__builtin_amdgcn_update_dpp(0, __float_as_int(v), 0x128, 0xf, 0xf, false));
  return sum_g(v);
}

template <int NWB_, int NNB_, int NT_, int NS_>
struct Cfg {
  static constexpr int NWB = NWB_, NNB = NNB_, NT = NT_, NS = NS_, MT = 4;
  static constexpr int A_BYTES = 16384, STAGE = A_BYTES + NWB * 8192 + NNB * 4096, XTRA = NS * STAGE, LDS = XTRA + 8192;
  static constexpr int L = 4 + 2 * NWB + NNB;
};
template <class C> struct Frags { bf16x8 a[C::MT]; bf16x8 b[C::NT]; };

DI bf16x8 rd_bfrag(lds_t p, int hi_off) {
  s16x4 lo = __builtin_amdgcn_ds_read_tr16_b64_v4i16((LAS s16x4*)p);
  s16x4 hi = __builtin_amdgcn_ds_read_tr16_b64_v4i16((LAS s16x4*)(p + hi_off));
  return __builtin_shufflevector(lo, hi, 0, 1, 2, 3, 4, 5, 6, 7);
}
template <class C, class P>
DI void read_frags(Frags<C>& F, int ks, const unsigned (&ad)[P::NBASE + 2]) {
#pragma unroll
  for (int i = 0; i < C::NT; ++i) {
    lds_t p = (lds_t)(size_t)ad[P::base(i)];
    F.b[i] = P::narrow(i) ? rd_bfrag(p + ks * 2048, 256) : rd_bfrag(p + P::imm(i) + ks * 4096, 512);
    if (i == 0) {
#pragma unroll
      for (int m = 0; m < C::MT; ++m) F.a[m] = *(const LAS bf16x8*)((lds_t)(size_t)ad[P::NBASE + ks] + m * 2048);
    }
  }
}
template <class C, class P, int LO, int HI>
DI void mma(f32x4 (&acc)[C::MT][C::NT], const Frags<C>& F) {
#pragma unroll
  for (int i = LO; i < HI; ++i) {
    const int n = i / C::MT, m = i % C::MT;
    acc[m][n] = P::trans(n) ? __builtin_amdgcn_mfma_f32_16x16x32_bf16(F.b[n], F.a[m], acc[m][n], 0, 0, 0)
                            : __builtin_amdgcn_mfma_f32_16x16x32_bf16(F.a[m], F.b[n], acc[m][n], 0, 0, 0);
  }
}
template <int NM, int NR, int NV>
DI void sched_hint() {
  constexpr int PV = NM > NR ? (NV + NM - NR - 1) / (NM - NR) : 0;
#pragma unroll
  for (int i = 0; i < NM; ++i) {
    __builtin_amdgcn_sched_group_barrier(0x8, 1, 0);
    if (i < NR) __builtin_amdgcn_sched_group_barrier(0x100, 1, 0);
    else if (NV) __builtin_amdgcn_sched_group_barrier(0x2, PV, 0);
  }
}
struct NoHook { static constexpr int TS = -1; DI void pre() const {} DI void post() const {} };
#define UNR 1
template <class C, class P, class H>
DI void consume(lds_t smem, int nt, f32x4 (&acc)[C::MT][C::NT], int wm, int lane, const unsigned (&boff)[P::NBASE], const H& h, u64 cs0_ = 0, char* ws = nullptr, int kid = 0) {
  constexpr int NM = C::MT * C::NT, NR = C::MT + 2 * C::NT, D = NM - NR, NV = P::NBASE + 2;
  const int fi = lane & 15, g = lane >> 4, f = (fi >> 1) & 7;
  unsigned ad[NV];
#pragma unroll
  for (int i = 0; i < P::NBASE; ++i) ad[i] = (unsigned)(size_t)smem + boff[i];
  ad[P::NBASE] = (unsigned)(size_t)smem + (unsigned)((wm * 64 + fi) * 128 + ((g ^ f) * 16));
  ad[P::NBASE + 1] = (unsigned)(size_t)smem + (unsigned)((wm * 64 + fi) * 128 + (((4 + g) ^ f) * 16));
#pragma unroll
  for (int m = 0; m < C::MT; ++m)
#pragma unroll
    for (int n = 0; n < C::NT; ++n) acc[m][n] = (f32x4){0.f, 0.f, 0.f, 0.f};
  Frags<C> F0, F1;
  wait_lgkm0();
  bar();
  read_frags<C, P>(F0, 0, ad);
  __builtin_amdgcn_sched_barrier(0);
  int rs = 0;
  auto rest = [&]() {
    rs = (rs + 1 == C::NS) ? 0 : rs + 1;
    const unsigned dl = rs ? (unsigned)C::STAGE : (unsigned)(-(C::NS - 1) * C::STAGE);
    read_frags<C, P>(F1, 1, ad);
    mma<C, P, 0, NM>(acc, F0);
#pragma unroll
    for (int i = 0; i < NV; ++i) { ad[i] += dl; asm volatile("" : "+v"(ad[i])); }
    mma<C, P, 0, D>(acc, F1);
    sched_hint<NM, NR, NV>();
    sched_hint<D, 0, 0>();
    __builtin_amdgcn_sched_barrier(0);
    wait_lgkm0();
  };
  rest();
  auto tile = [&](int t) {
    __builtin_amdgcn_sched_barrier(0);
    read_frags<C, P>(F0, 0, ad);
    mma<C, P, D, NM>(acc, F1);
    sched_hint<NM - D, NR, 0>();
    rest();
  };
  int t = 0;
  if (H::TS >= 0) {
#pragma unroll 1
    for (; t < H::TS; ++t) { bar(); tile(t); }
    h.pre(); bar(); h.post(); tile(t); ++t;
  }
#pragma unroll 1
  for (; t < nt - 1; ++t) { bar(); tile(t); }
  mma<C, P, D, NM>(acc, F1);
}
template <class C, class LD>
DI void produce(lds_t smem, int nt, LD& L, u64 cs0_ = 0, char* ws_ = nullptr, int kid = 0) {
  u64 c5_ = 0, c6_ = 0;
#pragma unroll
  for (int s = 0; s < C::NS - 1; ++s) { L.issue(smem + s * C::STAGE, s);
  }
  wait_vm<(C::NS - 2) * C::L>();
  bar();
  int ws = C::NS - 1;
  for (int t = 0; t < nt - C::NS + 1; ++t) {
    L.issue(smem + ws * C::STAGE, t + C::NS - 1);
    wait_vm<(C::NS - 2) * C::L>();
    ws = (ws + 1 == C::NS) ? 0 : ws + 1;
    bar();
  }
  L.tail(smem + ws * C::STAGE);
  if (C::NS == 4) { wait_vm<C::L + LD::NTAIL>(); bar(); }
  wait_vm<LD::NTAIL>();
  bar();
}
DI unsigned voff_a(int lane, int lw, unsigned lda2) { return (unsigned)(lane >> 3) * lda2 + (unsigned)(((lane & 7) ^ (((lw & 1) * 4 + (lane >> 4)) & 7)) * 16); }
DI unsigned voff_w(int lane, int lw, unsigned ldb2) { return (unsigned)(lane >> 3) * ldb2 + (unsigned)(((lane & 7) ^ (((lane >> 4) & 1) * 2 + (lw & 1) * 4)) * 16); }
DI unsigned voff_n(int lane, unsigned ldb2) { return (unsigned)(lane >> 2) * ldb2 + (unsigned)(((lane & 3) ^ (((lane >> 5) & 1) * 2)) * 16); }
DI void issue_a(rsrc_t rA, lds_t st, int lw, unsigned voff, unsigned soff  , unsigned lda2) {
#pragma unroll
  for (int i = 0; i < 4; ++i) glds16(rA, st + (lw + 4 * i) * 1024, voff, soff + (unsigned)(lw + 4 * i) * 8u * lda2);
}
DI void issue_w(rsrc_t rB, lds_t blk, int lw, unsigned voff, unsigned soff  , unsigned ldb2) {
#pragma unroll
  for (int i = 0; i < 2; ++i) glds16(rB, blk + (lw + 4 * i) * 1024, voff, soff + (unsigned)(lw + 4 * i) * 8u * ldb2);
}
DI unsigned roff_w(int lane, int j) {
  const int g = lane >> 4, q = (lane >> 2) & 3, pp = lane & 3, f = ((q >> 1) & 1) | ((g & 1) << 1);
  return (unsigned)((8 * g + q) * 128 + pp * 8 + ((j ^ f) << 5));
}
DI unsigned roff_n(int lane, int j) {
  const int g = lane >> 4, q = (lane >> 2) & 3, pp = lane & 3;
  return (unsigned)(g * 512 + q * 64 + pp * 8 + ((j ^ (g & 1)) << 5));
}

__global__ __launch_bounds__(512) void k0(const u16* __restrict__ x, const u16* __restrict__ wqkv, const u16* __restrict__ w1,
                                          const u16* __restrict__ gw, char* __restrict__ ws) {
  __shared__ float red[8][64];
  STAMP0();
  const int w = threadIdx.x >> 6, lane = threadIdx.x & 63, b = blockIdx.x;
  const brs_t rW = mk_brs(ws, WS_BYTES);
  const int r = b * 8 + w;
  const u32x4 x0 = *(const u32x4*)(x + (size_t)r * D_ + lane * 8), x1 = *(const u32x4*)(x + (size_t)r * D_ + 512 + lane * 8);
  const u32x4 wv = *(const u32x4*)(w1 + (size_t)((b >> 4) * 64 + w * 8 + (lane >> 3)) * D_ + (b & 15) * 64 + (lane & 7) * 8);
  const u32x4 g0 = *(const u32x4*)(gw + lane * 8), g1 = *(const u32x4*)(gw + 512 + lane * 8);
  unsigned touch = 0;
  if (lane < 16) touch = *(const unsigned*)(wqkv + (size_t)(b * 4 + (w >> 1)) * 3072 + 1024 + ((w & 1) * 16 + lane) * 64);
  float s = 0.f, a = 0.f;
#pragma unroll
  for (int e = 0; e < 4; ++e) {
    s = dot2(x0[e], ONES, s); s = dot2(x1[e], ONES, s);
    a = dot2(cvt_pk(bf_lo(x0[e]) * bf_lo(g0[e]), bf_hi(x0[e]) * bf_hi(g0[e])), ONES, a);
    a = dot2(cvt_pk(bf_lo(x1[e]) * bf_lo(g1[e]), bf_hi(x1[e]) * bf_hi(g1[e])), ONES, a);
  }
  s = sum_r8(s); a = sum_r8(a);
#pragma unroll
  for (int o = 1; o < 8; o <<= 1) { s += __shfl_xor(s, o); a += __shfl_xor(a, o); }
  const float mean = s * (1.f / 1024.f);
  float q = 0.f;
#pragma unroll
  for (int e = 0; e < 4; ++e) {
    float d;
    d = bf_lo(x0[e]) - mean; q = __builtin_fmaf(d, d, q); d = bf_hi(x0[e]) - mean; q = __builtin_fmaf(d, d, q);
    d = bf_lo(x1[e]) - mean; q = __builtin_fmaf(d, d, q); d = bf_hi(x1[e]) - mean; q = __builtin_fmaf(d, d, q);
  }
  q = sum_r8(q);
#pragma unroll
  for (int o = 1; o < 8; o <<= 1) q += __shfl_xor(q, o);
  const float rstd = 1.f / sqrtf(q * (1.f / 1024.f) + 1e-5f);
  const float gg = rbf(1.f / (1.f + __expf(-rbf(a))));
  if (lane == 0) {
    u32x2 v; v.x = __float_as_uint(gg); v.y = __float_as_uint(rbf(1.f - gg)); st8(rW, (unsigned)WS_G + r * 8, v);
    v.x = __float_as_uint(rstd); v.y = __float_as_uint(-mean * rstd); st8(rW, (unsigned)WS_RS1 + r * 8, v);
  }
#pragma unroll
  for (int e = 0; e < 4; ++e) {
    const float lo = sum_r8(bf_lo(wv[e])), hi = sum_r8(bf_hi(wv[e]));
    if (lane < 8) { red[w][lane * 8 + e * 2] = lo; red[w][lane * 8 + e * 2 + 1] = hi; }
  }
  __syncthreads();
  if (w == 0) {
    float c = 0.f;
#pragma unroll
    for (int i = 0; i < 8; ++i) c += red[i][lane];
    __builtin_amdgcn_raw_buffer_store_b32(__float_as_uint(c), rW, (int)((unsigned)WS_CS1 + ((b >> 4) * 1024 + (b & 15) * 64 + lane) * 4), 0, 16);
    if (lane == 0) { u32x2 z = {0u, 0u}; st8(rW, (unsigned)WS_FLAG + b * 128, z); __builtin_amdgcn_raw_buffer_store_b32(0u, rW, (int)((unsigned)WS_FLAG + 65536u + b * 4u), 0, 16); __builtin_amdgcn_raw_buffer_store_b32(0u, rW, (int)((unsigned)WS_FLAG + 65536u + 1024u + b * 4u), 0, 16); }
  }
  asm volatile("" ::"v"(touch));
  STAMP_END(4, 2);
}

typedef Cfg<3, 0, 6, 3> C1;
struct P1 {
  static constexpr int NBASE = 2;
  static constexpr bool trans(int n) { return n >= 4; }
  static constexpr bool narrow(int) { return false; }
  static constexpr int base(int i) { return i & 1; }
  static constexpr int imm(int i) { return (i >> 1) * 8192; }
};
struct L1 {
  static constexpr int NTAIL = 3;
  u64 c_[5];
  rsrc_t rA, rQ, rW, rS, rN; unsigned va, vq, vw, aoff, qcol, wcol; int lw, lane, pm, pn; lds_t xtra;
  DI void issue(lds_t st, int kt) {
    issue_a(rA, st, lw, va, aoff + (unsigned)kt * 128u, 2048u);
    issue_w(rQ, st + 16384, lw, vq, (unsigned)kt * (64u * 6144u) + qcol + 2048u, 6144u);
    issue_w(rQ, st + 16384 + 8192, lw, vq, (unsigned)kt * (64u * 6144u) + qcol + 4096u, 6144u);
    issue_w(rW, st + 16384 + 16384, lw, vw, (unsigned)kt * (64u * 2048u) + wcol, 2048u);
  }
  DI void tail(lds_t st) {
    glds16(rS, st + lw * 1024, (unsigned)((lw * 4 + (lane >> 4)) * 4096 + (lane & 15) * 16), (unsigned)WS_CS1 + pn * 256u);
    glds16(rS, xtra, (unsigned)lane * 16u, (unsigned)WS_RS1 + pm * 1024u);
    const unsigned line = (unsigned)(blockIdx.x * 64 + (lw & 1) * 32 + (lane & 31));
    gtouch((lw & 2) ? rN : rQ, xtra + 4096, (line >> 4) * ((lw & 2) ? 2048u : 6144u) + (line & 15) * 128u, 0u);
  }
};
__global__ __launch_bounds__(512, 2) void k1(const u16* __restrict__ x, const u16* __restrict__ wqkv, const u16* __restrict__ w1,
                                             const u16* __restrict__ w2, char* __restrict__ ws) {
  extern __shared__ __attribute__((aligned(16))) char smem_[];
  lds_t smem = (lds_t)smem_;
  typedef C1 C;
  STAMP0();
  const int w = __builtin_amdgcn_readfirstlane(threadIdx.x >> 6), lane = threadIdx.x & 63;
  const int xcd = blockIdx.x & 7, j = blockIdx.x >> 3;
  const int pm = (xcd & 1) * 8 + (j & 7), pn = (xcd >> 1) * 4 + (j >> 3);
  const int fi = lane & 15, g = lane >> 4;
  const brs_t rW = mk_brs(ws, WS_BYTES);
  LAS f32x2* xst = (LAS f32x2*)(smem + C::XTRA);
  LAS float* xcs = (LAS float*)(smem + C::XTRA + 1024);
  lds_t dump = smem + 2 * C::STAGE;
  if (w >= 4) {
    const int lw = w - 4;
    L1 L;
    L.rA = mk_rsrc(x, S_ * D_ * 2); L.rQ = mk_rsrc(wqkv, D_ * 3 * D_ * 2); L.rW = mk_rsrc(w1, D_ * D_ * 2); L.rS = mk_rsrc(ws, WS_BYTES); L.rN = mk_rsrc(w2, D_ * D_ * 2);
    L.lw = lw; L.lane = lane; L.pm = pm; L.pn = pn; L.xtra = smem + C::XTRA;
    L.va = voff_a(lane, lw, 2048u); L.vq = voff_w(lane, lw, 6144u); L.vw = voff_w(lane, lw, 2048u);
    L.aoff = (unsigned)pm * 128u * 2048u; L.qcol = (unsigned)pn * 128u; L.wcol = (unsigned)pn * 128u;
    produce<C>(smem, 16, L CARG(0));
    wait_vm<1>();
    {
      f32x4 c = *(const LAS f32x4*)(smem + C::STAGE + lw * 1024 + lane * 16);
#pragma unroll
      for (int e = 0; e < 4; ++e) c[e] = sum_g(c[e]);
      if (g == 0) *(LAS f32x4*)(xcs + lw * 64 + fi * 4) = c;
    }
    wait_lgkm0();
    bar();
    const int wm = lw >> 1, mh = lw & 1;
    f32x4 cs[4];
#pragma unroll
    for (int n = 0; n < 4; ++n) {
      cs[n] = *(const LAS f32x4*)(xcs + n * 16 + g * 4);
#pragma unroll
      for (int i = 1; i < 4; ++i) cs[n] += *(const LAS f32x4*)(xcs + i * 64 + n * 16 + g * 4);
    }
    const int cg = pn >> 2, sh = (cg & 2) ? 2 : 1, dl = (cg & 1) ? -sh : sh;
    const unsigned hs = (unsigned)WS_HS + (pn * 64 + g * 16 + (fi & 1) * 8) * 2;
#pragma unroll
    for (int mm = 0; mm < 2; ++mm) {
      const int m = mh * 2 + mm, lr0 = wm * 64 + m * 16;
      const f32x2 rs = xst[lr0 + fi];
      unsigned p[4][2];
#pragma unroll
      for (int n = 0; n < 4; ++n) {
        const f32x4 a = *(const LAS f32x4*)(dump + (((wm * 2 + (n >> 1)) * 4 + m) * 2 + (n & 1)) * 1024 + lane * 16);
        float h[4];
#pragma unroll
        for (int r = 0; r < 4; ++r) h[r] = gelu_t(__builtin_fmaf(a[r], rs.x, rs.y * cs[n][r]));
        p[n][0] = cvt_pk(h[0], h[1]); p[n][1] = cvt_pk(h[2], h[3]);
      }
      u32x4 a, b;
      pack_quad(p, a, b, fi & 1);
      const int d0 = pm * 128 + lr0 + (fi & 14) + dl, d1 = d0 + 1;
      if ((unsigned)d0 < (unsigned)S_) st16(rW, hs + d0 * 2048, a);
      if ((unsigned)d1 < (unsigned)S_) st16(rW, hs + d1 * 2048, b);
    }
    if (dl > 0 ? (pm == 0 && lw == 0) : (pm == 15 && lw == 3)) {
      if ((fi >> 1) < sh) { const int zr = dl > 0 ? (fi >> 1) : S_ - 1 - (fi >> 1); const u32x4 zz = {0u, 0u, 0u, 0u}; st16(rW, hs + zr * 2048, zz); }
    }
    LST_OUT2(0, L);
    STAMP_END(0, 2);
    return;
  }
  const int wm = w >> 1, wn = w & 1;
  unsigned boff[2];
  boff[0] = 16384 + roff_w(lane, 2 * wn); boff[1] = 16384 + roff_w(lane, 2 * wn + 1);
  f32x4 acc[4][6];
  consume<C, P1>(smem, 16, acc, wm, lane, boff, NoHook() CARG(0));
  STAMP(0, 1); STAMPC(0);
#pragma unroll
  for (int m = 0; m < 4; ++m)
#pragma unroll
    for (int n = 0; n < 2; ++n) *(LAS f32x4*)(dump + ((w * 4 + m) * 2 + n) * 1024 + lane * 16) = acc[m][4 + n];
  wait_lgkm0();
  bar();
  {
    const int head = pn * 2 + wn, part = pm * 2 + wm;
    f32x4 kv[2][2], z[2];
#pragma unroll
    for (int T = 0; T < 2; ++T) { z[T] = (f32x4){0.f, 0.f, 0.f, 0.f}; kv[T][0] = z[T]; kv[T][1] = z[T]; }
    const u32x4 onesv = {ONES, ONES, ONES, ONES};
#pragma unroll
    for (int mp = 0; mp < 2; ++mp) {
      u32x4 pk[2], vv[2];
#pragma unroll
      for (int T = 0; T < 2; ++T) {
        const f32x4 a = acc[2 * mp][T], b = acc[2 * mp + 1][T];
        pk[T].x = cvt_pk(phi_f(a[0]), phi_f(a[1])); pk[T].y = cvt_pk(phi_f(a[2]), phi_f(a[3]));
        pk[T].z = cvt_pk(phi_f(b[0]), phi_f(b[1])); pk[T].w = cvt_pk(phi_f(b[2]), phi_f(b[3]));
        const f32x4 c = acc[2 * mp][2 + T], d = acc[2 * mp + 1][2 + T];
        vv[T].x = cvt_pk(c[0], c[1]); vv[T].y = cvt_pk(c[2], c[3]); vv[T].z = cvt_pk(d[0], d[1]); vv[T].w = cvt_pk(d[2], d[3]);
      }
#pragma unroll
      for (int T = 0; T < 2; ++T) {
#pragma unroll
        for (int U = 0; U < 2; ++U)
          kv[T][U] = __builtin_amdgcn_mfma_f32_16x16x32_bf16(__builtin_bit_cast(bf16x8, pk[T]), __builtin_bit_cast(bf16x8, vv[U]), kv[T][U], 0, 0, 0);
        z[T] = __builtin_amdgcn_mfma_f32_16x16x32_bf16(__builtin_bit_cast(bf16x8, pk[T]), __builtin_bit_cast(bf16x8, onesv), z[T], 0, 0, 0);
      }
    }
    const unsigned kp = (unsigned)WS_KVP + (head * 32 + part) * 4096 + lane * 16;
#pragma unroll
    for (int T = 0; T < 2; ++T)
#pragma unroll
      for (int U = 0; U < 2; ++U) st16f(rW, kp + (T * 2 + U) * 1024, kv[T][U]);
    if (fi == 0) {
#pragma unroll
      for (int T = 0; T < 2; ++T) st16f(rW, (unsigned)WS_ZP + (head * 32 + part) * 128 + T * 64 + g * 16, z[T]);
    }
  }
  STAMP_END(0, 2);
}

typedef Cfg<2, 0, 4, 4> C2;
constexpr int LDS2 = C2::XTRA + 32768;
struct P2 {
  static constexpr int NBASE = 4;
  static constexpr bool trans(int) { return true; }
  static constexpr bool narrow(int) { return false; }
  static constexpr int base(int i) { return i; }
  static constexpr int imm(int) { return 0; }
};
struct L2 {
  rsrc_t rA, rB; unsigned va, vb, aoff, ldb2, bcol; int lw; lds_t smem;
  DI void issue(int kt) {
    lds_t st = smem + (kt & 3) * C2::STAGE;
    issue_a(rA, st, lw, va, aoff + (unsigned)kt * 128u, 2048u);
    issue_w(rB, st + 16384, lw, vb, (unsigned)kt * 64u * ldb2 + bcol, ldb2);
    issue_w(rB, st + 16384 + 8192, lw, vb, (unsigned)kt * 64u * ldb2 + bcol + 128u, ldb2);
  }
};
template <int TS_> struct FlagHook {
  static constexpr int TS = TS_;
  brs_t r; unsigned off; bool me;
  DI void pre() const {}
  DI void post() const { if (me) __builtin_amdgcn_raw_buffer_store_b32(1u, r, (int)off, 0, 16); }
};
DI void await_flags(unsigned v, const char* fp) {
  if (__builtin_amdgcn_ballot_w64(v == 0u) == 0ull) return;
  for (int it = 0; it < (1 << 18); ++it) {
    asm volatile("global_load_dword %0, %1, off sc1\n\ts_waitcnt vmcnt(0)" : "=v"(v) : "v"(fp) : "memory");
    if (__builtin_amdgcn_ballot_w64(v == 0u) == 0ull) break;
    __builtin_amdgcn_s_sleep(1);
  }
}
__global__ __launch_bounds__(512, 2) void k2(const u16* __restrict__ x, const u16* __restrict__ wqkv, const u16* __restrict__ w2,
                                             const u16* __restrict__ w3, char* __restrict__ ws) {
  extern __shared__ __attribute__((aligned(16))) char smem_[];
  lds_t smem = (lds_t)smem_;
  typedef C2 C;
  STAMP0();
  const int w = __builtin_amdgcn_readfirstlane(threadIdx.x >> 6), lane = threadIdx.x & 63;
  const int xcd = blockIdx.x & 7, j = blockIdx.x >> 3;
  const bool isq = xcd < 4;
  const int pm = (xcd & 1) * 8 + (j & 7), pn = ((xcd >> 1) & 1) * 4 + (j >> 3);
  const int fi = lane & 15, g = lane >> 4;
  const brs_t rW = mk_brs(ws, WS_BYTES);
  lds_t X = smem + C::XTRA;
  if (w >= 4) {
    const int lw = w - 4;
    const unsigned l16 = (unsigned)lane * 16u;
    L2 L;
    L.rA = mk_rsrc(isq ? (const void*)x : (const void*)(ws + WS_HS), S_ * D_ * 2);
    L.rB = isq ? mk_rsrc(wqkv, D_ * 3 * D_ * 2) : mk_rsrc(w2, D_ * D_ * 2);
    L.lw = lw; L.ldb2 = isq ? 6144u : 2048u; L.smem = smem;
    L.va = voff_a(lane, lw, 2048u); L.vb = voff_w(lane, lw, L.ldb2);
    L.aoff = (unsigned)pm * 128u * 2048u; L.bcol = (unsigned)pn * 256u;
    const rsrc_t rS = mk_rsrc(ws, WS_BYTES);
    const int kh = blockIdx.x >> 3, kTU = (blockIdx.x >> 1) & 3, khalf = blockIdx.x & 1;
    LST(c1_);
    L.issue(0);
    LST(c5_);
    L.issue(1);
#pragma unroll
    for (int i = 0; i < 4; ++i)
      glds16(rS, X + (lw * 4 + i) * 1024, (unsigned)(lane >> 5) * 4096u + (lane & 31) * 16u, (unsigned)WS_KVP + ((kh * 32 + lw * 8 + 2 * i) * 4 + kTU) * 1024u + khalf * 512u);
    LST(c6_);
    L.issue(2);
    LST(c2_);
    wait_vm<20>();
    LST(c3_);
    bar();
    L.issue(3); wait_vm<16>(); bar();
    L.issue(4);
    {
      lds_t p = X + (lane >> 3) * 512 + (lw * 8 + (lane & 7)) * 16;
      f32x4 s = *(const LAS f32x4*)p;
#pragma unroll
      for (int i = 1; i < 4; ++i) s += *(const LAS f32x4*)(p + i * 4096);
#pragma unroll
      for (int e = 0; e < 4; ++e) s[e] = sum_r8(s[e]);
      u32x2 v; v.x = cvt_pk(s[0], s[1]); v.y = cvt_pk(s[2], s[3]);
      if (lane < 8) st8(rW, (unsigned)WS_KVF + (kh * 2 + (kTU & 1)) * 1024 + (kTU >> 1) * 512 + (khalf * 32 + lw * 8 + lane) * 8, v);
    }
    wait_vm<17>(); bar();
    L.issue(5); wait_vm<17>(); bar();
    L.issue(6); wait_vm<17>(); bar();
    for (int t = 7; t < 14; ++t) { L.issue(t); wait_vm<16>(); bar(); }
    L.issue(14);
    const unsigned foff = (unsigned)WS_FLAG + 65536u + pn * 128u + (lane & 31) * 4u;
    gpoll(rS, X + 28672 + lw * 256, foff);
    wait_vm<17>(); bar();
    L.issue(15); wait_vm<17>(); bar();
    wait_vm<8>();
    const int u = blockIdx.x < 224 ? blockIdx.x : blockIdx.x - 224, slab = u % 56, rg = (u / 56) * 4 + lw;
    const rsrc_t rN = mk_rsrc(w3, D_ * F_ * 2);
    const unsigned noff = (unsigned)rg * (64u * 7168u) + slab * 128u, nv = (unsigned)(lane >> 3) * 7168u + (lane & 7) * 16u;
    if (isq) {
      await_flags(*(const LAS unsigned*)(X + 28672 + lw * 256 + lane * 4), ws + foff);
      const int head = pn * 4 + lw;
#pragma unroll
      for (int i = 0; i < 2; ++i) glds16c(rS, X + (lw * 2 + i) * 1024, l16, (unsigned)WS_KVF + (head * 2 + i) * 1024u);
#pragma unroll
      for (int i = 0; i < 4; ++i) glds16(rS, X + 8192 + (lw * 4 + i) * 1024, l16, (unsigned)WS_ZP + head * 4096u + i * 1024u);
      glds16(rS, X + 24576, l16, (unsigned)WS_G + pm * 1024u);
      bar();
#pragma unroll
      for (int i = 0; i < 8; ++i) glds16(rN, smem + (lw * 8 + i) * 1024, nv, noff + i * (8u * 7168u));
      wait_vm<15>(); bar();
      wait_vm<8>();
      f32x4 z = *(const LAS f32x4*)(X + 8192 + lw * 4096 + l16);
#pragma unroll
      for (int i = 1; i < 4; ++i) z += *(const LAS f32x4*)(X + 8192 + lw * 4096 + i * 1024 + l16);
#pragma unroll
      for (int e = 0; e < 4; ++e) z[e] = sum_r8(z[e]);
      u32x2 v; v.x = cvt_pk(z[0], z[1]); v.y = cvt_pk(z[2], z[3]);
      if (lane < 8) *(LAS u32x2*)(X + 25600 + lw * 64 + (lane & 3) * 16 + (lane >> 2) * 8) = v;
      wait_lgkm0();
    } else {
      const rsrc_t rX = mk_rsrc(x, S_ * D_ * 2);
#pragma unroll
      for (int i = 0; i < 8; ++i) {
        const int r = (lw * 8 + i) * 4;
        glds16(rX, X + r * 256, (unsigned)g * 2048u + (unsigned)((fi ^ ((r + g) & 15)) * 16), (unsigned)(pm * 128 + r) * 2048u + pn * 256u);
      }
      bar();
#pragma unroll
      for (int i = 0; i < 8; ++i) glds16(rN, smem + (lw * 8 + i) * 1024, nv, noff + i * (8u * 7168u));
      wait_vm<16>(); bar();
      wait_vm<8>();
    }
    bar();
    wait_vm<0>();
    float c[8];
#pragma unroll
    for (int e = 0; e < 8; ++e) c[e] = 0.f;
#pragma unroll
    for (int i = 0; i < 8; ++i) {
      const u32x4 v = *(const LAS u32x4*)(smem + (lw * 8 + i) * 1024 + lane * 16);
#pragma unroll
      for (int e = 0; e < 4; ++e) { c[2 * e] += bf_lo(v[e]); c[2 * e + 1] += bf_hi(v[e]); }
    }
#pragma unroll
    for (int e = 0; e < 8; ++e) c[e] = sum_r8(c[e]);
    if (lane < 8 && blockIdx.x < 224) {
      const unsigned o = (unsigned)WS_CS3 + (rg * F_ + slab * 64 + lane * 8) * 4;
      st16f(rW, o, (f32x4){c[0], c[1], c[2], c[3]}); st16f(rW, o + 16, (f32x4){c[4], c[5], c[6], c[7]});
    }
    LST_OUT(1);
    STAMP_END(1, 2);
    return;
  }
  const int wm = w >> 1, wn = w & 1;
  unsigned boff[4];
#pragma unroll
  for (int i = 0; i < 4; ++i) boff[i] = 16384 + wn * 8192 + roff_w(lane, i);
  f32x4 acc[4][4];
  FlagHook<4> fh; fh.r = rW; fh.off = (unsigned)WS_FLAG + 65536u + blockIdx.x * 4u; fh.me = threadIdx.x == 0;
  consume<C, P2>(smem, 16, acc, wm, lane, boff, fh CARG(1));
  STAMP(1, 1); STAMPC(1);
  bar();
  const int row0 = pm * 128 + wm * 64, col0 = pn * 128 + wn * 64 + g * 16 + (fi & 1) * 8;
  if (isq) {
    bf16x8 kf[2][2], zf[2];
#pragma unroll
    for (int h = 0; h < 2; ++h) {
#pragma unroll
      for (int U = 0; U < 2; ++U) {
        lds_t kp = X + (wn * 2 + h) * 2048 + U * 1024 + lane * 8;
        kf[h][U] = __builtin_shufflevector(*(const LAS s16x4*)kp, *(const LAS s16x4*)(kp + 512), 0, 1, 2, 3, 4, 5, 6, 7);
      }
      zf[h] = *(const LAS bf16x8*)(X + 25600 + (wn * 2 + h) * 64 + g * 16);
    }
#pragma unroll
    for (int m = 0; m < 4; ++m) {
      const float gg = *(const LAS float*)(X + 24576 + (wm * 64 + m * 16 + fi) * 8);
      unsigned p[4][2];
#pragma unroll
      for (int h = 0; h < 2; ++h) {
        const f32x4 a = acc[m][2 * h], b = acc[m][2 * h + 1], zero = {0.f, 0.f, 0.f, 0.f};
        u32x4 q; q.x = cvt_pk(phi_f(a[0]), phi_f(a[1])); q.y = cvt_pk(phi_f(a[2]), phi_f(a[3])); q.z = cvt_pk(phi_f(b[0]), phi_f(b[1])); q.w = cvt_pk(phi_f(b[2]), phi_f(b[3]));
        const bf16x8 pq = __builtin_bit_cast(bf16x8, q);
        const f32x4 n0 = __builtin_amdgcn_mfma_f32_16x16x32_bf16(kf[h][0], pq, zero, 0, 0, 0);
        const f32x4 n1 = __builtin_amdgcn_mfma_f32_16x16x32_bf16(kf[h][1], pq, zero, 0, 0, 0);
        const f32x4 dn = __builtin_amdgcn_mfma_f32_16x16x32_bf16(zf[h], pq, zero, 0, 0, 0);
        const float sc = gg / (dn[0] + 1e-6f);
        p[2 * h][0] = cvt_pk(n0[0] * sc, n0[1] * sc); p[2 * h][1] = cvt_pk(n0[2] * sc, n0[3] * sc);
        p[2 * h + 1][0] = cvt_pk(n1[0] * sc, n1[1] * sc); p[2 * h + 1][1] = cvt_pk(n1[2] * sc, n1[3] * sc);
      }
      store_quad(rW, (unsigned)WS_AU + ((row0 + m * 16 + (fi & 14)) * LDAU + col0) * 2, LDAU * 2, p, fi & 1);
    }
  } else {
    lds_t xl = X + (wm * 64 + fi) * 256 + (g & 1) * 8;
#pragma unroll
    for (int m = 0; m < 4; ++m) {
      float s1 = 0.f, s2 = 0.f;
      unsigned p[4][2];
#pragma unroll
      for (int n = 0; n < 4; ++n) {
        const u32x2 xr = *(const LAS u32x2*)(xl + m * 4096 + (((wn * 8 + n * 2 + (g >> 1)) ^ fi) * 16));
        p[n][0] = cvt_pk(acc[m][n][0] + bf_lo(xr.x), acc[m][n][1] + bf_hi(xr.x));
        p[n][1] = cvt_pk(acc[m][n][2] + bf_lo(xr.y), acc[m][n][3] + bf_hi(xr.y));
        s1 = dot2(p[n][0], ONES, s1); s1 = dot2(p[n][1], ONES, s1);
        s2 = dot2(p[n][0], p[n][0], s2); s2 = dot2(p[n][1], p[n][1], s2);
      }
      store_quad(rW, (unsigned)WS_X1 + ((row0 + m * 16 + (fi & 14)) * D_ + col0) * 2, 2048, p, fi & 1);
      s1 = sum_g(s1); s2 = sum_g(s2);
      if (g == 0) { u32x2 v; v.x = __float_as_uint(s1); v.y = __float_as_uint(s2); st8(rW, (unsigned)WS_ST + (pm * 16 + pn * 2 + wn) * 1024 + (wm * 64 + m * 16 + fi) * 8, v); }
    }
  }
  STAMP_END(1, 2);
}

typedef Cfg<3, 1, 7, 3> C3;
struct P3 {
  static constexpr int NBASE = 7;
  static constexpr bool trans(int) { return true; }
  static constexpr bool narrow(int i) { return i == 6; }
  static constexpr int base(int i) { return i; }
  static constexpr int imm(int) { return 0; }
};
struct L3 {
  static constexpr int NTAIL = 11;
  u64 c_[5];
  rsrc_t rA, rB, rS, rO, rF; unsigned va, vb, vn, aoff, wcol, ncol; int lw, lane, pm, pn; lds_t xtra;
  DI void issue(lds_t st, int kt) {
    issue_a(rA, st, lw, va, aoff + (unsigned)kt * 128u, 2048u);
    const unsigned kb = (unsigned)kt * (64u * 7168u);
#pragma unroll
    for (int b = 0; b < 3; ++b) issue_w(rB, st + 16384 + b * 8192, lw, vb, kb + wcol + b * 128u, 7168u);
    glds16(rB, st + 16384 + 24576 + lw * 1024, vn, kb + ncol + (unsigned)lw * (16u * 7168u));
  }
  DI void tail(lds_t st) {
    st += lw * 9216;
    const unsigned l16 = (unsigned)lane * 16u;
#pragma unroll
    for (int i = 0; i < 4; ++i) glds16(rS, st + i * 1024, (unsigned)(lane >> 4) * 1024u + (lane & 15) * 16u, (unsigned)WS_ST + (pm * 16 + i * 4) * 1024u + lw * 256u);
#pragma unroll
    for (int i = 0; i < 4; ++i) glds16(rS, st + 4096 + i * 1024, l16, (unsigned)WS_CS3 + ((lw * 4 + i) * F_ + pn * 224) * 4u);
    glds16(rS, st + 8192, l16, (unsigned)WS_G + pm * 1024u);
    const unsigned lo = (unsigned)((blockIdx.x * 4 + lw) * 16 + (lane & 15)), lf = (unsigned)((blockIdx.x * 4 + lw) * 56 + (lane < 56 ? lane : 55));
    gtouch(rF, xtra + 6144, lf * 128u, 0u);
    gtouch(rO, xtra + 6144, lo * 128u, 0u);
  }
};
typedef Cfg<2, 0, 4, 4> C4;
struct L4 {
  rsrc_t rA, rO, rW; unsigned va, vb, aoff, bcol; int lw, hf, own; lds_t smem;
  DI void issue(int t) {
    lds_t st = smem + (t & 3) * C4::STAGE;
    const bool o = t < 8;
    const int r = t - 11, kb = o ? hf * 8 + t : hf * 28 + (t < 11 ? own + t - 8 : r + (r >= own ? 3 : 0));
    const unsigned so = aoff + (unsigned)(o ? kb : kb + 16) * 128u;
#pragma unroll
    for (int i = 0; i < 4; ++i) glds16c(rA, st + (lw + 4 * i) * 1024, va, so + (unsigned)(lw + 4 * i) * (8u * LDAU * 2u));
    rsrc_t rB; rB.x = o ? rO.x : rW.x; rB.y = o ? rO.y : rW.y; rB.z = o ? rO.z : rW.z; rB.w = rO.w;
    issue_w(rB, st + 16384, lw, vb, (unsigned)kb * (64u * 2048u) + bcol, 2048u);
    issue_w(rB, st + 16384 + 8192, lw, vb, (unsigned)kb * (64u * 2048u) + bcol + 128u, 2048u);
  }
};
#define TSU 4
#define TPOLL 10
struct DrainHook : FlagHook<TSU> { DI void pre() const { wait_vm<0>(); } };
__global__ __launch_bounds__(512, 2) void k3(const u16* __restrict__ w3, const u16* __restrict__ wo, const u16* __restrict__ w4,
                                             char* __restrict__ ws, u16* __restrict__ out) {
  extern __shared__ __attribute__((aligned(16))) char smem_[];
  lds_t smem = (lds_t)smem_;
  const int w = __builtin_amdgcn_readfirstlane(threadIdx.x >> 6), lane = threadIdx.x & 63;
  const int xcd = blockIdx.x & 7, j = blockIdx.x >> 3;
  const int pm = (xcd & 1) * 8 + (j & 7), pn = (xcd >> 1) * 4 + (j >> 3);
  const int hf = pn >> 3, pn4 = pn & 7;
  const int fi = lane & 15, g = lane >> 4;
  const brs_t rW = mk_brs(ws, WS_BYTES);
  const int wc0 = (pn & 1) ? 32 : 0, nc0 = (pn & 1) ? 0 : 192;
  LAS f32x4* rst = (LAS f32x4*)(smem + C3::XTRA);
  LAS float* xcs = (LAS float*)(smem + C3::XTRA + 2048);
  constexpr unsigned UFLAG = (unsigned)WS_FLAG + 65536u + 1024u;
  if (w >= 4) {
    const int lw = w - 4;
    const rsrc_t rS = mk_rsrc(ws, WS_BYTES), rO = mk_rsrc(wo, D_ * D_ * 2), rF = mk_rsrc(w4, F_ * D_ * 2);
    {
      typedef C3 C;
      STAMP0();
      L3 L;
      L.rA = mk_rsrc(ws + WS_X1, S_ * D_ * 2); L.rB = mk_rsrc(w3, D_ * F_ * 2); L.rS = rS; L.rO = rO; L.rF = rF;
      L.lw = lw; L.lane = lane; L.pm = pm; L.pn = pn; L.xtra = smem + C::XTRA;
      L.va = voff_a(lane, lw, 2048u); L.vb = voff_w(lane, lw, 7168u); L.vn = voff_n(lane, 7168u);
      L.aoff = (unsigned)pm * 128u * 2048u; L.wcol = (unsigned)(pn * 224 + wc0) * 2u; L.ncol = (unsigned)(pn * 224 + nc0) * 2u;
      produce<C>(smem, 16, L CARG(2));
      wait_vm<2>();
      lds_t st = smem + C::STAGE + lw * 9216;
      f32x4 s = *(const LAS f32x4*)(st + lane * 16), c = *(const LAS f32x4*)(st + 4096 + lane * 16);
#pragma unroll
      for (int i = 1; i < 4; ++i) { s += *(const LAS f32x4*)(st + i * 1024 + lane * 16); c += *(const LAS f32x4*)(st + 4096 + i * 1024 + lane * 16); }
#pragma unroll
      for (int e = 0; e < 4; ++e) s[e] = sum_g(s[e]);
      const int lr = lw * 32 + fi * 2 + (g & 1);
      const float omg = *(const LAS float*)(st + 8192 + lr * 8 + 4);
      const float mean = ((g & 1) ? s[2] : s[0]) * (1.f / 1024.f);
      const float var = fmaxf(((g & 1) ? s[3] : s[1]) * (1.f / 1024.f) - mean * mean, 0.f);
      const float rstd = 1.f / sqrtf(var + 1e-5f);
      if (g < 2) rst[lr] = (f32x4){-mean, -2.30220819814f * rstd, -0.10294323970f * rstd * rstd * rstd, fminf(1.f / (rstd * omg), 1e30f)};
      *(LAS f32x4*)(xcs + lw * 256 + lane * 4) = c;
      wait_lgkm0();
      bar();
      LST_OUT2(2, L);
      STAMP_NW(2, 2);
    }
    STAMP0();
    L4 L;
    L.rA = mk_rsrc(ws + WS_AU, S_ * LDAU * 2); L.rO = rO; L.rW = rF; L.smem = smem;
    L.lw = lw; L.hf = hf; L.own = (7 * pn4 + 1) >> 1; L.va = voff_a(lane, lw, LDAU * 2u); L.vb = voff_w(lane, lw, 2048u);
    L.aoff = (unsigned)pm * 128u * (LDAU * 2u); L.bcol = (unsigned)pn4 * 256u;
    LST(c1_); L.issue(0); LST(c5_); L.issue(1); LST(c6_); L.issue(2); LST(c2_);
    wait_vm<16>();
    LST(c3_);
    bar();
    int t = 3;
#pragma unroll 1
    for (; t < TPOLL; ++t) { L.issue(t); wait_vm<16>(); bar(); }
    const unsigned foff = UFLAG + (pm * 16 + hf * 8 + (lane & 7)) * 4u;
    lds_t fl = smem + C3::XTRA + 6144 + 256 + lw * 256;
    gpoll(rS, fl, foff);
#pragma unroll 1
    for (; t < 11; ++t) { L.issue(t); wait_vm<16>(); bar(); }
    wait_vm<8 * (11 - TPOLL)>();
    await_flags(*(const LAS unsigned*)(fl + lane * 4), ws + foff);
#pragma unroll 1
    for (; t < 36; ++t) { L.issue(t); wait_vm<16>(); bar(); }
    wait_vm<8>(); bar();
    wait_vm<0>(); bar();
    LST_OUT(3);
    STAMP_END(3, 2);
    return;
  }
  const int wm = w >> 1, wn = w & 1;
  {
    typedef C3 C;
    STAMP0();
    unsigned boff[7];
#pragma unroll
    for (int i = 0; i < 4; ++i) boff[i] = 16384 + wn * 8192 + roff_w(lane, i);
    boff[4] = 16384 + 16384 + roff_w(lane, 2 * wn); boff[5] = 16384 + 16384 + roff_w(lane, 2 * wn + 1);
    boff[6] = 16384 + 24576 + roff_n(lane, wn);
    f32x4 acc[4][7];
    consume<C, P3>(smem, 16, acc, wm, lane, boff, NoHook() CARG(2));
    STAMP(2, 1); STAMPC(2);
    lds_t xch = smem + C::XTRA + 8192;
#pragma unroll
    for (int i = 0; i < 2; ++i) {
      f32x4 snd;
#pragma unroll
      for (int r = 0; r < 4; ++r) snd[r] = wn ? acc[i][6][r] : acc[2 + i][6][r];
      *(LAS f32x4*)(xch + (w * 2 + i) * 1024 + lane * 16) = snd;
    }
    wait_lgkm0();
    bar();
    f32x4 cs[8], an[4];
#pragma unroll
    for (int i = 0; i < 2; ++i) {
      const f32x4 o = *(const LAS f32x4*)(xch + ((w ^ 1) * 2 + i) * 1024 + lane * 16);
#pragma unroll
      for (int r = 0; r < 4; ++r) { const float own = wn ? acc[2 + i][6][r] : acc[i][6][r]; an[2 * i][r] = wn ? o[r] : own; an[2 * i + 1][r] = wn ? own : o[r]; }
    }
#pragma unroll
    for (int n = 0; n < 8; ++n) {
      const int c0 = (n < 4 ? wc0 + wn * 64 + n * 16 : n < 6 ? wc0 + 128 + wn * 32 + (n - 4) * 16 : nc0 + (n - 6) * 16) + g * 4;
      cs[n] = *(const LAS f32x4*)(xcs + c0);
#pragma unroll
      for (int i = 1; i < 4; ++i) cs[n] += *(const LAS f32x4*)(xcs + i * 256 + c0);
    }
    auto act = [](const f32x4& a4, const f32x4& c4, const f32x4& rs, unsigned (&p)[2]) {
      const f32x2 nm = {rs[0], rs[0]}, c0 = {rs[1], rs[1]}, c1 = {rs[2], rs[2]}, q = {rs[3], rs[3]};
#pragma unroll
      for (int h = 0; h < 2; ++h) {
        const f32x2 a = {a4[2 * h], a4[2 * h + 1]}, c = {c4[2 * h], c4[2 * h + 1]};
        const f32x2 t = __builtin_elementwise_fma(c, nm, a);
        const f32x2 y = t * __builtin_elementwise_fma(t * t, c1, c0);
        const f32x2 e = {__builtin_amdgcn_exp2f(y[0]), __builtin_amdgcn_exp2f(y[1])};
        const f32x2 d = __builtin_elementwise_fma(e, q, q);
        const f32x2 o = t * (f32x2){__builtin_amdgcn_rcpf(d[0]), __builtin_amdgcn_rcpf(d[1])};
        p[h] = cvt_pk(o[0], o[1]);
      }
    };
    const unsigned urow = (unsigned)WS_AU + ((pm * 128 + wm * 64 + (fi & 14)) * LDAU + 1024 + pn * 224 + (fi & 1) * 8) * 2;
    const unsigned u2 = urow + ((g >> 1) * 16 * LDAU + (g & 1) * 16) * 2;
    unsigned p2[4][2];
#pragma unroll
    for (int m = 0; m < 4; ++m) {
      const f32x4 rs = rst[wm * 64 + m * 16 + fi];
      unsigned p[4][2];
#pragma unroll
      for (int n = 0; n < 4; ++n) act(acc[m][n], cs[n], rs, p[n]);
      store_quad(rW, urow + (m * 16 * LDAU + wc0 + wn * 64 + g * 16) * 2, LDAU * 2, p, fi & 1);
      act(acc[m][4], cs[4], rs, p2[(m & 1) * 2]); act(acc[m][5], cs[5], rs, p2[(m & 1) * 2 + 1]);
      if (m & 1) store_quad(rW, u2 + ((m - 1) * 16 * LDAU + wc0 + 128 + wn * 32) * 2, LDAU * 2, p2, fi & 1);
    }
#pragma unroll
    for (int i = 0; i < 2; ++i) {
      const f32x4 rs = rst[wm * 64 + (wn * 2 + i) * 16 + fi];
      act(an[2 * i], cs[6], rs, p2[2 * i]); act(an[2 * i + 1], cs[7], rs, p2[2 * i + 1]);
    }
    store_quad(rW, u2 + (wn * 32 * LDAU + nc0) * 2, LDAU * 2, p2, fi & 1);
    STAMP_NW(2, 2);
  }
  typedef C4 C;
  STAMP0();
  const int row0 = pm * 128 + wm * 64 + hf * 32;
  const unsigned eoff = (unsigned)((row0 + (fi & 14)) * D_ + pn4 * 128 + wn * 64 + g * 16 + (fi & 1) * 8) * 2;
  u32x4 xr[2][2]; float omg[2];
#pragma unroll
  for (int m = 0; m < 2; ++m) {
#pragma unroll
    for (int p = 0; p < 2; ++p) xr[m][p] = *(const u32x4*)(ws + WS_X1 + eoff + (m * 16 + p) * 2048);
    omg[m] = *(const float*)(ws + WS_G + (size_t)(row0 + m * 16 + fi) * 8 + 4);
  }
  unsigned boff[4];
#pragma unroll
  for (int i = 0; i < 4; ++i) boff[i] = 16384 + wn * 8192 + roff_w(lane, i);
  f32x4 acc[4][4];
  DrainHook dh; dh.r = rW; dh.off = UFLAG + (pm * 16 + pn) * 4u; dh.me = threadIdx.x == 0;
  consume<C, P2>(smem, 36, acc, wm, lane, boff, dh CARG(3));
  STAMP(3, 1); STAMPC(3);
  f32x4 own[2][4];
  {
    const unsigned ob = (unsigned)WS_XCH + (blockIdx.x * 4 + w) * 8192 + lane * 16;
#pragma unroll
    for (int m = 0; m < 2; ++m)
#pragma unroll
      for (int n = 0; n < 4; ++n) {
        f32x4 snd;
#pragma unroll
        for (int r = 0; r < 4; ++r) { snd[r] = hf ? acc[m][n][r] : acc[2 + m][n][r]; own[m][n][r] = hf ? acc[2 + m][n][r] : acc[m][n][r]; }
        st16f(rW, ob + (m * 4 + n) * 1024, snd);
      }
    wait_vm<0>();
    STAMP(3, 3);
    bar();
    if (threadIdx.x == 0) __builtin_amdgcn_raw_buffer_store_b32(1u, rW, (int)((unsigned)WS_FLAG + blockIdx.x * 128), 0, 16);
  }
  {
    await_flags(0u, ws + WS_FLAG + (size_t)(blockIdx.x ^ 4) * 128);
    STAMP(3, 4);
    const unsigned ib = (unsigned)WS_XCH + ((blockIdx.x ^ 4) * 4 + w) * 8192 + lane * 16;
    f32x4 o[2][4];
#pragma unroll
    for (int m = 0; m < 2; ++m)
#pragma unroll
      for (int n = 0; n < 4; ++n) o[m][n] = __builtin_bit_cast(f32x4, __builtin_amdgcn_raw_buffer_load_b128(rW, (int)(ib + (m * 4 + n) * 1024), 0, 16));
    const brs_t rO = mk_brs(out, S_ * D_ * 2);
#pragma unroll
    for (int m = 0; m < 2; ++m) {
      unsigned p[4][2];
      unpack_quad(xr[m][0], xr[m][1], p, fi & 1);
      const float q = omg[m];
#pragma unroll
      for (int n = 0; n < 4; ++n) {
        const f32x4 a = own[m][n] + o[m][n];
        p[n][0] = cvt_pk(__builtin_fmaf(q, bf_lo(p[n][0]), a[0]), __builtin_fmaf(q, bf_hi(p[n][0]), a[1]));
        p[n][1] = cvt_pk(__builtin_fmaf(q, bf_lo(p[n][1]), a[2]), __builtin_fmaf(q, bf_hi(p[n][1]), a[3]));
      }
      store_quad(rO, eoff + m * 32768, 2048, p, fi & 1);
    }
  }
  STAMP_END(3, 2);
}

extern "C" __global__ void kend() {}

extern "C" void kernel_launch(void* const* d_in, const int* in_sizes, int n_in, void* d_out, int out_size, void* d_ws, size_t ws_size,
                              hipStream_t stream) {
  const u16* x = (const u16*)d_in[0];
  const u16* wqkv = (const u16*)d_in[1];
  const u16* wo = (const u16*)d_in[2];
  const u16* w1 = (const u16*)d_in[4];
  const u16* w2 = (const u16*)d_in[5];
  const u16* w3 = (const u16*)d_in[6];
  const u16* w4 = (const u16*)d_in[7];
  const u16* gw = (const u16*)d_in[8];
  char* ws = (char*)d_ws + WSOFF;
  (void)hipFuncSetAttribute((const void*)k1, hipFuncAttributeMaxDynamicSharedMemorySize, C1::LDS);
  (void)hipFuncSetAttribute((const void*)k2, hipFuncAttributeMaxDynamicSharedMemorySize, LDS2);
  (void)hipFuncSetAttribute((const void*)k3, hipFuncAttributeMaxDynamicSharedMemorySize, C3::LDS + 8192);
  k0<<<256, 512, 0, stream>>>(x, wqkv, w1, gw, ws);
  k1<<<256, 512, C1::LDS, stream>>>(x, wqkv, w1, w2, ws);
  k2<<<256, 512, LDS2, stream>>>(x, wqkv, w2, w3, ws);
  k3<<<256, 512, C3::LDS + 8192, stream>>>(w3, wo, w4, ws, (u16*)d_out);
}
```

```cpp
#include <hip/hip_runtime.h>

typedef unsigned short u16;
typedef unsigned long long u64;
typedef short bf16x8 __attribute__((ext_vector_type(8)));
typedef short s16x4 __attribute__((ext_vector_type(4)));
typedef float f32x4 __attribute__((ext_vector_type(4)));
typedef float f32x2 __attribute__((ext_vector_type(2)));
typedef unsigned u32x4 __attribute__((ext_vector_type(4)));
typedef unsigned u32x2 __attribute__((ext_vector_type(2)));
typedef __bf16 bf16x2_t __attribute__((ext_vector_type(2)));
#define LAS __attribute__((address_space(3)))
typedef LAS char* lds_t;
typedef u32x4 rsrc_t;
#define DI __device__ __forceinline__

constexpr int S_ = 2048, D_ = 1024, F_ = 3584;
constexpr int LDAU = 4608 + 64;
constexpr size_t MB = 1u << 20;
constexpr size_t WS_HS = 0, WS_PQ = 4 * MB, WS_X1 = 8 * MB, WS_AU = 12 * MB, WS_KVP = 32 * MB, WS_ZP = 36 * MB,
                 WS_KVF = 36 * MB + 512 * 1024, WS_ZF = 36 * MB + 768 * 1024, WS_ST = 37 * MB, WS_G = 37 * MB + 512 * 1024,
                 WS_RS1 = 37 * MB + 640 * 1024, WS_CS1 = 37 * MB + 768 * 1024, WS_FLAG = 38 * MB, WS_CS3 = 39 * MB, WS_XCH = 40 * MB;

DI rsrc_t mk_rsrc(const void* p, unsigned bytes) {
  u64 a = (u64)p; rsrc_t r; r.x = (unsigned)a; r.y = (unsigned)(a >> 32) & 0xffffu; r.z = bytes; r.w = 0x00020000u; return r;
}
DI void glds16(rsrc_t r, lds_t dst, unsigned voff, unsigned soff) {
  asm volatile("s_mov_b32 m0, %0\n\ts_nop 0\n\tbuffer_load_dwordx4 %1, %2, %3 offen lds" ::"s"((unsigned)(size_t)dst), "v"(voff), "s"(r), "s"(soff) : "memory");
}
DI void gtouch(rsrc_t r, lds_t dst, unsigned voff, unsigned soff) {
  asm volatile("s_mov_b32 m0, %0\n\ts_nop 0\n\tbuffer_load_dword %1, %2, %3 offen lds" ::"s"((unsigned)(size_t)dst), "v"(voff), "s"(r), "s"(soff) : "memory");
}
DI void glds16c(rsrc_t r, lds_t dst, unsigned voff, unsigned soff) {
  asm volatile("s_mov_b32 m0, %0\n\ts_nop 0\n\tbuffer_load_dwordx4 %1, %2, %3 offen sc1 lds" ::"s"((unsigned)(size_t)dst), "v"(voff), "s"(r), "s"(soff) : "memory");
}
DI void gpoll(rsrc_t r, lds_t dst, unsigned voff) {
  asm volatile("s_mov_b32 m0, %0\n\ts_nop 0\n\tbuffer_load_dword %1, %2, 0 offen sc1 lds" ::"s"((unsigned)(size_t)dst), "v"(voff), "s"(r) : "memory");
}
template <int N> DI void wait_vm() { asm volatile("s_waitcnt vmcnt(%0)" ::"n"(N) : "memory"); }
DI void wait_lgkm0() { asm volatile("" ::: "memory"); __builtin_amdgcn_s_waitcnt(0xC07F); asm volatile("" ::: "memory"); }
DI void bar() { asm volatile("" ::: "memory"); __builtin_amdgcn_s_barrier(); asm volatile("" ::: "memory"); }
DI unsigned cvt_pk(float lo, float hi) { f32x2 v = {lo, hi}; bf16x2_t r = __builtin_convertvector(v, bf16x2_t); return __builtin_bit_cast(unsigned, r); }
DI float bf_lo(unsigned u) { return __builtin_bit_cast(float, u << 16); }
DI float bf_hi(unsigned u) { return __builtin_bit_cast(float, u & 0xffff0000u); }
DI float rbf(float f) { return bf_lo(cvt_pk(f, 0.f) & 0xffffu); }
DI float dot2(unsigned a, unsigned b, float c) { return __builtin_amdgcn_fdot2_f32_bf16(__builtin_bit_cast(bf16x2_t, a), __builtin_bit_cast(bf16x2_t, b), c, false); }
constexpr unsigned ONES = 0x3f803f80u;
typedef __amdgpu_buffer_rsrc_t brs_t;
constexpr unsigned WS_BYTES = 48u << 20;
DI brs_t mk_brs(void* p, unsigned bytes) { return __builtin_amdgcn_make_buffer_rsrc(p, (short)0, (int)bytes, 0x00020000); }
#define CT 0
#define WSOFF 0
#define STOFF (WSOFF ? -(long)(WSOFF) : (long)(64 * MB))
#define SAUX 16
DI void st16(brs_t r, unsigned off, u32x4 v) { __builtin_amdgcn_raw_buffer_store_b128(v, r, (int)off, 0, SAUX); }
DI void st8(brs_t r, unsigned off, u32x2 v) { __builtin_amdgcn_raw_buffer_store_b64(v, r, (int)off, 0, SAUX); }
DI void st16f(brs_t r, unsigned off, f32x4 v) { __builtin_amdgcn_raw_buffer_store_b128(__builtin_bit_cast(u32x4, v), r, (int)off, 0, 16); }
#define STAMP(kid, i) do {} while (0)
#define STAMP_END(kid, i) do {} while (0)
#define CARG(k)
#define LST(v) do {} while (0)
#define LST_OUT(kid) do {} while (0)
#define LST_OUT2(kid, L) do {} while (0)
#define STAMP_NW(kid, i) do {} while (0)
#define STAMP0() do {} while (0)
#define STAMPC(kid) do {} while (0)
#define CODE_TOUCH(E, i) do { u64 pc_; unsigned len_; asm volatile("s_getpc_b64 %0\n1:\n\ts_mov_b32 %1, " E "-1b" : "=s"(pc_), "=s"(len_)); \
    const unsigned o_ = (unsigned)(i) * 128u; if (o_ < len_) { unsigned t_; asm volatile("global_load_dword %0, %1, %2" : "=v"(t_) : "v"(o_), "s"(pc_) : "memory"); ct_ = t_; } } while (0)
DI float sum_g(float v) {
  unsigned u = __builtin_bit_cast(unsigned, v);
  auto a = __builtin_amdgcn_permlane16_swap(u, u, false, false);
  const unsigned a0 = a[0], a1 = a[1];
  v = __uint_as_float(a0) + __uint_as_float(a1);
  u = __builtin_bit_cast(unsigned, v);
  auto b = __builtin_amdgcn_permlane32_swap(u, u, false, false);
  const unsigned b0 = b[0], b1 = b[1];
  return __uint_as_float(b0) + __uint_as_float(b1);
}
DI float gelu_t(float v) {
  const float e = __builtin_amdgcn_exp2f(v * __builtin_fmaf(v * v, -0.10294323970f, -2.30220819814f));
  return v * __builtin_amdgcn_rcpf(1.f + e);
}
DI float phi_f(float t) { return (t > 0.f ? t + 1.f : __builtin_amdgcn_exp2f(t * 1.44269504089f)) + 1e-6f; }
DI u32x4 pair_swap(unsigned a0, unsigned a1, unsigned b0, unsigned b1) {
  auto r0 = __builtin_amdgcn_permlane16_swap(a0, b0, false, false);
  auto r1 = __builtin_amdgcn_permlane16_swap(a1, b1, false, false);
  u32x4 v; v.x = r0[0]; v.y = r1[0]; v.z = r0[1]; v.w = r1[1]; return v;
}
DI int quad_col(int lane) { const int g = lane >> 4; return ((lane >> 3) & 1) * 32 + (g & 1) * 16 + (g & 2) * 4; }
DI void row_xchg(u32x4& a, u32x4& b) {
#pragma unroll
  for (int e = 0; e < 4; ++e) {
    const unsigned x = a[e], y = b[e];
    a[e] = (unsigned)__builtin_amdgcn_update_dpp((int)x, (int)y, 0x128, 0xf, 0xc, false);
    b[e] = (unsigned)__builtin_amdgcn_update_dpp((int)y, (int)x, 0x128, 0xf, 0x3, false);
  }
}
DI void store_quad(brs_t r, unsigned off  , unsigned pitch8  , const unsigned (&p)[4][2]) {
  u32x4 a = pair_swap(p[0][0], p[0][1], p[1][0], p[1][1]), b = pair_swap(p[2][0], p[2][1], p[3][0], p[3][1]);
  row_xchg(a, b);
  st16(r, off, a); st16(r, off + pitch8, b);
}
DI void unpack_quad(u32x4 a, u32x4 b, unsigned (&p)[4][2]) {
  row_xchg(a, b);
  const u32x4 x = pair_swap(a.x, a.y, a.z, a.w), y = pair_swap(b.x, b.y, b.z, b.w);
  p[0][0] = x.x; p[0][1] = x.y; p[1][0] = x.z; p[1][1] = x.w; p[2][0] = y.x; p[2][1] = y.y; p[3][0] = y.z; p[3][1] = y.w;
}
DI float sum_r8(float v) {
  v += __int_as_float(__builtin_amdgcn_update_dpp(0, __float_as_int(v), 0x128, 0xf, 0xf, false));
  return sum_g(v);
}

template <int NWB_, int NNB_, int NT_, int NS_>
struct Cfg {
  static constexpr int NWB = NWB_, NNB = NNB_, NT = NT_, NS = NS_, MT = 4;
  static constexpr int A_BYTES = 16384, STAGE = A_BYTES + NWB * 8192 + NNB * 4096, XTRA = NS * STAGE, LDS = XTRA + 8192;
  static constexpr int L = 4 + 2 * NWB + NNB;
};
template <class C> struct Frags { bf16x8 a[C::MT]; bf16x8 b[C::NT]; };

DI bf16x8 rd_bfrag(lds_t p, int hi_off) {
  s16x4 lo = __builtin_amdgcn_ds_read_tr16_b64_v4i16((LAS s16x4*)p);
  s16x4 hi = __builtin_amdgcn_ds_read_tr16_b64_v4i16((LAS s16x4*)(p + hi_off));
  return __builtin_shufflevector(lo, hi, 0, 1, 2, 3, 4, 5, 6, 7);
}
template <class C, class P>
DI void read_frags(Frags<C>& F, int ks, const unsigned (&ad)[P::NBASE + 2]) {
#pragma unroll
  for (int i = 0; i < C::NT; ++i) {
    lds_t p = (lds_t)(size_t)ad[P::base(i)];
    F.b[i] = P::narrow(i) ? rd_bfrag(p + ks * 2048, 256) : rd_bfrag(p + P::imm(i) + ks * 4096, 512);
    if (i == 0) {
#pragma unroll
      for (int m = 0; m < C::MT; ++m) F.a[m] = *(const LAS bf16x8*)((lds_t)(size_t)ad[P::NBASE + ks] + m * 2048);
    }
  }
}
template <class C, class P, int LO, int HI>
DI void mma(f32x4 (&acc)[C::MT][C::NT], const Frags<C>& F) {
#pragma unroll
  for (int i = LO; i < HI; ++i) {
    const int n = i / C::MT, m = i % C::MT;
    acc[m][n] = P::trans(n) ? __builtin_amdgcn_mfma_f32_16x16x32_bf16(F.b[n], F.a[m], acc[m][n], 0, 0, 0)
                            : __builtin_amdgcn_mfma_f32_16x16x32_bf16(F.a[m], F.b[n], acc[m][n], 0, 0, 0);
  }
}
template <int NM, int NR, int NV>
DI void sched_hint() {
  constexpr int PV = NM > NR ? (NV + NM - NR - 1) / (NM - NR) : 0;
#pragma unroll
  for (int i = 0; i < NM; ++i) {
    __builtin_amdgcn_sched_group_barrier(0x8, 1, 0);
    if (i < NR) __builtin_amdgcn_sched_group_barrier(0x100, 1, 0);
    else if (NV) __builtin_amdgcn_sched_group_barrier(0x2, PV, 0);
  }
}
struct NoHook { static constexpr int TS = -1; DI void pre() const {} DI void post() const {} };
#define UNR 1
template <class C, class P, class H>
DI void consume(lds_t smem, int nt, f32x4 (&acc)[C::MT][C::NT], int wm, int lane, const unsigned (&boff)[P::NBASE], const H& h, u64 cs0_ = 0, char* ws = nullptr, int kid = 0) {
  constexpr int NM = C::MT * C::NT, NR = C::MT + 2 * C::NT, D = NM - NR, NV = P::NBASE + 2;
  const int fi = lane & 15, g = lane >> 4, f = (fi >> 1) & 7;
  unsigned ad[NV];
#pragma unroll
  for (int i = 0; i < P::NBASE; ++i) ad[i] = (unsigned)(size_t)smem + boff[i];
  ad[P::NBASE] = (unsigned)(size_t)smem + (unsigned)((wm * 64 + fi) * 128 + ((g ^ f) * 16));
  ad[P::NBASE + 1] = (unsigned)(size_t)smem + (unsigned)((wm * 64 + fi) * 128 + (((4 + g) ^ f) * 16));
#pragma unroll
  for (int m = 0; m < C::MT; ++m)
#pragma unroll
    for (int n = 0; n < C::NT; ++n) acc[m][n] = (f32x4){0.f, 0.f, 0.f, 0.f};
  Frags<C> F0, F1;
  wait_lgkm0();
  bar();
  read_frags<C, P>(F0, 0, ad);
  __builtin_amdgcn_sched_barrier(0);
  int rs = 0;
  auto rest = [&]() {
    rs = (rs + 1 == C::NS) ? 0 : rs + 1;
    const unsigned dl = rs ? (unsigned)C::STAGE : (unsigned)(-(C::NS - 1) * C::STAGE);
    read_frags<C, P>(F1, 1, ad);
    mma<C, P, 0, NM>(acc, F0);
#pragma unroll
    for (int i = 0; i < NV; ++i) { ad[i] += dl; asm volatile("" : "+v"(ad[i])); }
    mma<C, P, 0, D>(acc, F1);
    sched_hint<NM, NR, NV>();
    sched_hint<D, 0, 0>();
    __builtin_amdgcn_sched_barrier(0);
    wait_lgkm0();
  };
  rest();
  auto tile = [&](int t) {
    __builtin_amdgcn_sched_barrier(0);
    read_frags<C, P>(F0, 0, ad);
    mma<C, P, D, NM>(acc, F1);
    sched_hint<NM - D, NR, 0>();
    rest();
  };
  int t = 0;
  if (H::TS >= 0) {
#pragma unroll 1
    for (; t < H::TS; ++t) { bar(); tile(t); }
    h.pre(); bar(); h.post(); tile(t); ++t;
  }
#pragma unroll 1
  for (; t < nt - 1; ++t) { bar(); tile(t); }
  mma<C, P, D, NM>(acc, F1);
}
template <class C, class LD>
DI void produce(lds_t smem, int nt, LD& L, u64 cs0_ = 0, char* ws_ = nullptr, int kid = 0) {
  u64 c5_ = 0, c6_ = 0;
#pragma unroll
  for (int s = 0; s < C::NS - 1; ++s) { L.issue(smem + s * C::STAGE, s);
  }
  wait_vm<(C::NS - 2) * C::L>();
  bar();
  int ws = C::NS - 1;
  for (int t = 0; t < nt - C::NS + 1; ++t) {
    L.issue(smem + ws * C::STAGE, t + C::NS - 1);
    wait_vm<(C::NS - 2) * C::L>();
    ws = (ws + 1 == C::NS) ? 0 : ws + 1;
    bar();
  }
  L.tail(smem + ws * C::STAGE);
  if (C::NS == 4) { wait_vm<C::L + LD::NTAIL>(); bar(); }
  wait_vm<LD::NTAIL>();
  bar();
}
DI unsigned voff_a(int lane, int lw, unsigned lda2) { return (unsigned)(lane >> 3) * lda2 + (unsigned)(((lane & 7) ^ (((lw & 1) * 4 + (lane >> 4)) & 7)) * 16); }
DI unsigned voff_w(int lane, int lw, unsigned ldb2) { return (unsigned)(lane >> 3) * ldb2 + (unsigned)(((lane & 7) ^ (((lane >> 4) & 1) * 2 + (lw & 1) * 4)) * 16); }
DI unsigned voff_n(int lane, unsigned ldb2) { return (unsigned)(lane >> 2) * ldb2 + (unsigned)(((lane & 3) ^ (((lane >> 5) & 1) * 2)) * 16); }
DI void issue_a(rsrc_t rA, lds_t st, int lw, unsigned voff, unsigned soff  , unsigned lda2) {
#pragma unroll
  for (int i = 0; i < 4; ++i) glds16(rA, st + (lw + 4 * i) * 1024, voff, soff + (unsigned)(lw + 4 * i) * 8u * lda2);
}
DI void issue_w(rsrc_t rB, lds_t blk, int lw, unsigned voff, unsigned soff  , unsigned ldb2) {
#pragma unroll
  for (int i = 0; i < 2; ++i) glds16(rB, blk + (lw + 4 * i) * 1024, voff, soff + (unsigned)(lw + 4 * i) * 8u * ldb2);
}
DI unsigned roff_w(int lane, int j) {
  const int g = lane >> 4, q = (lane >> 2) & 3, pp = lane & 3, f = ((q >> 1) & 1) | ((g & 1) << 1);
  return (unsigned)((8 * g + q) * 128 + pp * 8 + ((j ^ f) << 5));
}
DI unsigned roff_n(int lane, int j) {
  const int g = lane >> 4, q = (lane >> 2) & 3, pp = lane & 3;
  return (unsigned)(g * 512 + q * 64 + pp * 8 + ((j ^ (g & 1)) << 5));
}

__global__ __launch_bounds__(512) void k0(const u16* __restrict__ x, const u16* __restrict__ wqkv, const u16* __restrict__ w1,
                                          const u16* __restrict__ gw, char* __restrict__ ws) {
  __shared__ float red[8][64];
  STAMP0();
  const int w = threadIdx.x >> 6, lane = threadIdx.x & 63, b = blockIdx.x;
  const brs_t rW = mk_brs(ws, WS_BYTES);
  const int r = b * 8 + w;
  const u32x4 x0 = *(const u32x4*)(x + (size_t)r * D_ + lane * 8), x1 = *(const u32x4*)(x + (size_t)r * D_ + 512 + lane * 8);
  const u32x4 wv = *(const u32x4*)(w1 + (size_t)((b >> 4) * 64 + w * 8 + (lane >> 3)) * D_ + (b & 15) * 64 + (lane & 7) * 8);
  const u32x4 g0 = *(const u32x4*)(gw + lane * 8), g1 = *(const u32x4*)(gw + 512 + lane * 8);
  unsigned touch = 0;
  if (lane < 16) touch = *(const unsigned*)(wqkv + (size_t)(b * 4 + (w >> 1)) * 3072 + 1024 + ((w & 1) * 16 + lane) * 64);
  float s = 0.f, a = 0.f;
#pragma unroll
  for (int e = 0; e < 4; ++e) {
    s = dot2(x0[e], ONES, s); s = dot2(x1[e], ONES, s);
    a = dot2(cvt_pk(bf_lo(x0[e]) * bf_lo(g0[e]), bf_hi(x0[e]) * bf_hi(g0[e])), ONES, a);
    a = dot2(cvt_pk(bf_lo(x1[e]) * bf_lo(g1[e]), bf_hi(x1[e]) * bf_hi(g1[e])), ONES, a);
  }
  s = sum_r8(s); a = sum_r8(a);
#pragma unroll
  for (int o = 1; o < 8; o <<= 1) { s += __shfl_xor(s, o); a += __shfl_xor(a, o); }
  const float mean = s * (1.f / 1024.f);
  float q = 0.f;
#pragma unroll
  for (int e = 0; e < 4; ++e) {
    float d;
    d = bf_lo(x0[e]) - mean; q = __builtin_fmaf(d, d, q); d = bf_hi(x0[e]) - mean; q = __builtin_fmaf(d, d, q);
    d = bf_lo(x1[e]) - mean; q = __builtin_fmaf(d, d, q); d = bf_hi(x1[e]) - mean; q = __builtin_fmaf(d, d, q);
  }
  q = sum_r8(q);
#pragma unroll
  for (int o = 1; o < 8; o <<= 1) q += __shfl_xor(q, o);
  const float rstd = 1.f / sqrtf(q * (1.f / 1024.f) + 1e-5f);
  const float gg = rbf(1.f / (1.f + __expf(-rbf(a))));
  if (lane == 0) {
    u32x2 v; v.x = __float_as_uint(gg); v.y = __float_as_uint(rbf(1.f - gg)); st8(rW, (unsigned)WS_G + r * 8, v);
    v.x = __float_as_uint(rstd); v.y = __float_as_uint(-mean * rstd); st8(rW, (unsigned)WS_RS1 + r * 8, v);
  }
#pragma unroll
  for (int e = 0; e < 4; ++e) {
    const float lo = sum_r8(bf_lo(wv[e])), hi = sum_r8(bf_hi(wv[e]));
    if (lane < 8) { red[w][lane * 8 + e * 2] = lo; red[w][lane * 8 + e * 2 + 1] = hi; }
  }
  __syncthreads();
  if (w == 0) {
    float c = 0.f;
#pragma unroll
    for (int i = 0; i < 8; ++i) c += red[i][lane];
    __builtin_amdgcn_raw_buffer_store_b32(__float_as_uint(c), rW, (int)((unsigned)WS_CS1 + ((b >> 4) * 1024 + (b & 15) * 64 + lane) * 4), 0, 16);
    if (lane == 0) { u32x2 z = {0u, 0u}; st8(rW, (unsigned)WS_FLAG + b * 128, z); __builtin_amdgcn_raw_buffer_store_b32(0u, rW, (int)((unsigned)WS_FLAG + 65536u + b * 4u), 0, 16); __builtin_amdgcn_raw_buffer_store_b32(0u, rW, (int)((unsigned)WS_FLAG + 65536u + 1024u + b * 4u), 0, 16); }
  }
  asm volatile("" ::"v"(touch));
  STAMP_END(4, 2);
}

typedef Cfg<3, 0, 6, 3> C1;
struct P1 {
  static constexpr int NBASE = 2;
  static constexpr bool trans(int n) { return n >= 4; }
  static constexpr bool narrow(int) { return false; }
  static constexpr int base(int i) { return i & 1; }
  static constexpr int imm(int i) { return (i >> 1) * 8192; }
};
struct L1 {
  static constexpr int NTAIL = 3;
  u64 c_[5];
  rsrc_t rA, rQ, rW, rS, rN; unsigned va, vq, vw, aoff, qcol, wcol; int lw, lane, pm, pn; lds_t xtra;
  DI void issue(lds_t st, int kt) {
    issue_a(rA, st, lw, va, aoff + (unsigned)kt * 128u, 2048u);
    issue_w(rQ, st + 16384, lw, vq, (unsigned)kt * (64u * 6144u) + qcol + 2048u, 6144u);
    issue_w(rQ, st + 16384 + 8192, lw, vq, (unsigned)kt * (64u * 6144u) + qcol + 4096u, 6144u);
    issue_w(rW, st + 16384 + 16384, lw, vw, (unsigned)kt * (64u * 2048u) + wcol, 2048u);
  }
  DI void tail(lds_t st) {
    glds16(rS, st + lw * 1024, (unsigned)((lw * 4 + (lane >> 4)) * 4096 + (lane & 15) * 16), (unsigned)WS_CS1 + pn * 256u);
    glds16(rS, xtra, (unsigned)lane * 16u, (unsigned)WS_RS1 + pm * 1024u);
    const unsigned line = (unsigned)(blockIdx.x * 64 + (lw & 1) * 32 + (lane & 31));
    gtouch((lw & 2) ? rN : rQ, xtra + 4096, (line >> 4) * ((lw & 2) ? 2048u : 6144u) + (line & 15) * 128u, 0u);
  }
};
__global__ __launch_bounds__(512, 2) void k1(const u16* __restrict__ x, const u16* __restrict__ wqkv, const u16* __restrict__ w1,
                                             const u16* __restrict__ w2, char* __restrict__ ws) {
  extern __shared__ __attribute__((aligned(16))) char smem_[];
  lds_t smem = (lds_t)smem_;
  typedef C1 C;
  STAMP0();
  const int w = __builtin_amdgcn_readfirstlane(threadIdx.x >> 6), lane = threadIdx.x & 63;
  const int xcd = blockIdx.x & 7, j = blockIdx.x >> 3;
  const int pm = (xcd & 1) * 8 + (j & 7), pn = (xcd >> 1) * 4 + (j >> 3);
  const int fi = lane & 15, g = lane >> 4;
  const brs_t rW = mk_brs(ws, WS_BYTES);
  LAS f32x2* xst = (LAS f32x2*)(smem + C::XTRA);
  LAS float* xcs = (LAS float*)(smem + C::XTRA + 1024);
  lds_t dump = smem + 2 * C::STAGE;
  if (w >= 4) {
    const int lw = w - 4;
    L1 L;
    L.rA = mk_rsrc(x, S_ * D_ * 2); L.rQ = mk_rsrc(wqkv, D_ * 3 * D_ * 2); L.rW = mk_rsrc(w1, D_ * D_ * 2); L.rS = mk_rsrc(ws, WS_BYTES); L.rN = mk_rsrc(w2, D_ * D_ * 2);
    L.lw = lw; L.lane = lane; L.pm = pm; L.pn = pn; L.xtra = smem + C::XTRA;
    L.va = voff_a(lane, lw, 2048u); L.vq = voff_w(lane, lw, 6144u); L.vw = voff_w(lane, lw, 2048u);
    L.aoff = (unsigned)pm * 128u * 2048u; L.qcol = (unsigned)pn * 128u; L.wcol = (unsigned)pn * 128u;
    produce<C>(smem, 16, L CARG(0));
    wait_vm<1>();
    {
      f32x4 c = *(const LAS f32x4*)(smem + C::STAGE + lw * 1024 + lane * 16);
#pragma unroll
      for (int e = 0; e < 4; ++e) c[e] = sum_g(c[e]);
      if (g == 0) *(LAS f32x4*)(xcs + lw * 64 + fi * 4) = c;
    }
    wait_lgkm0();
    bar();
    const int wm = lw >> 1, mh = lw & 1;
    f32x4 cs[4];
#pragma unroll
    for (int n = 0; n < 4; ++n) {
      cs[n] = *(const LAS f32x4*)(xcs + n * 16 + g * 4);
#pragma unroll
      for (int i = 1; i < 4; ++i) cs[n] += *(const LAS f32x4*)(xcs + i * 64 + n * 16 + g * 4);
    }
    const int cg = pn >> 2, sh = (cg & 2) ? 2 : 1, dl = (cg & 1) ? -sh : sh;
    const unsigned hs = (unsigned)WS_HS + (pn * 64 + quad_col(lane)) * 2;
#pragma unroll
    for (int mm = 0; mm < 2; ++mm) {
      const int m = mh * 2 + mm, lr0 = wm * 64 + m * 16;
      const f32x2 rs = xst[lr0 + fi];
      unsigned p[4][2];
#pragma unroll
      for (int n = 0; n < 4; ++n) {
        const f32x4 a = *(const LAS f32x4*)(dump + (((wm * 2 + (n >> 1)) * 4 + m) * 2 + (n & 1)) * 1024 + lane * 16);
        float h[4];
#pragma unroll
        for (int r = 0; r < 4; ++r) h[r] = gelu_t(__builtin_fmaf(a[r], rs.x, rs.y * cs[n][r]));
        p[n][0] = cvt_pk(h[0], h[1]); p[n][1] = cvt_pk(h[2], h[3]);
      }
      u32x4 a = pair_swap(p[0][0], p[0][1], p[1][0], p[1][1]), b = pair_swap(p[2][0], p[2][1], p[3][0], p[3][1]);
      row_xchg(a, b);
      const int d0 = pm * 128 + lr0 + (fi & 7) + dl, d1 = d0 + 8;
      if (d0 >= 0) st16(rW, hs + d0 * 2048, a);
      if (d1 < S_) st16(rW, hs + d1 * 2048, b);
    }
    if (dl > 0 ? (pm == 0 && lw == 0) : (pm == 15 && lw == 3)) {
      if ((fi & 7) < sh) { const int zr = dl > 0 ? (fi & 7) : S_ - 1 - (fi & 7); const u32x4 zz = {0u, 0u, 0u, 0u}; st16(rW, hs + zr * 2048, zz); }
    }
    LST_OUT2(0, L);
    STAMP_END(0, 2);
    return;
  }
  const int wm = w >> 1, wn = w & 1;
  unsigned boff[2];
  boff[0] = 16384 + roff_w(lane, 2 * wn); boff[1] = 16384 + roff_w(lane, 2 * wn + 1);
  f32x4 acc[4][6];
  consume<C, P1>(smem, 16, acc, wm, lane, boff, NoHook() CARG(0));
  STAMP(0, 1); STAMPC(0);
#pragma unroll
  for (int m = 0; m < 4; ++m)
#pragma unroll
    for (int n = 0; n < 2; ++n) *(LAS f32x4*)(dump + ((w * 4 + m) * 2 + n) * 1024 + lane * 16) = acc[m][4 + n];
  wait_lgkm0();
  bar();
  {
    const int head = pn * 2 + wn, part = pm * 2 + wm;
    f32x4 kv[2][2], z[2];
#pragma unroll
    for (int T = 0; T < 2; ++T) { z[T] = (f32x4){0.f, 0.f, 0.f, 0.f}; kv[T][0] = z[T]; kv[T][1] = z[T]; }
    const u32x4 onesv = {ONES, ONES, ONES, ONES};
#pragma unroll
    for (int mp = 0; mp < 2; ++mp) {
      u32x4 pk[2], vv[2];
#pragma unroll
      for (int T = 0; T < 2; ++T) {
        const f32x4 a = acc[2 * mp][T], b = acc[2 * mp + 1][T];
        pk[T].x = cvt_pk(phi_f(a[0]), phi_f(a[1])); pk[T].y = cvt_pk(phi_f(a[2]), phi_f(a[3]));
        pk[T].z = cvt_pk(phi_f(b[0]), phi_f(b[1])); pk[T].w = cvt_pk(phi_f(b[2]), phi_f(b[3]));
        const f32x4 c = acc[2 * mp][2 + T], d = acc[2 * mp + 1][2 + T];
        vv[T].x = cvt_pk(c[0], c[1]); vv[T].y = cvt_pk(c[2], c[3]); vv[T].z = cvt_pk(d[0], d[1]); vv[T].w = cvt_pk(d[2], d[3]);
      }
#pragma unroll
      for (int T = 0; T < 2; ++T) {
#pragma unroll
        for (int U = 0; U < 2; ++U)
          kv[T][U] = __builtin_amdgcn_mfma_f32_16x16x32_bf16(__builtin_bit_cast(bf16x8, pk[T]), __builtin_bit_cast(bf16x8, vv[U]), kv[T][U], 0, 0, 0);
        z[T] = __builtin_amdgcn_mfma_f32_16x16x32_bf16(__builtin_bit_cast(bf16x8, pk[T]), __builtin_bit_cast(bf16x8, onesv), z[T], 0, 0, 0);
      }
    }
    const unsigned kp = (unsigned)WS_KVP + (head * 32 + part) * 4096 + lane * 16;
#pragma unroll
    for (int T = 0; T < 2; ++T)
#pragma unroll
      for (int U = 0; U < 2; ++U) st16f(rW, kp + (T * 2 + U) * 1024, kv[T][U]);
    if (fi == 0) {
#pragma unroll
      for (int T = 0; T < 2; ++T) st16f(rW, (unsigned)WS_ZP + (head * 32 + part) * 128 + T * 64 + g * 16, z[T]);
    }
  }
  STAMP_END(0, 2);
}

typedef Cfg<2, 0, 4, 4> C2;
constexpr int LDS2 = C2::XTRA + 32768;
struct P2 {
  static constexpr int NBASE = 4;
  static constexpr bool trans(int) { return true; }
  static constexpr bool narrow(int) { return false; }
  static constexpr int base(int i) { return i; }
  static constexpr int imm(int) { return 0; }
};
struct L2 {
  rsrc_t rA, rB; unsigned va, vb, aoff, ldb2, bcol; int lw; lds_t smem;
  DI void issue(int kt) {
    lds_t st = smem + (kt & 3) * C2::STAGE;
    issue_a(rA, st, lw, va, aoff + (unsigned)kt * 128u, 2048u);
    issue_w(rB, st + 16384, lw, vb, (unsigned)kt * 64u * ldb2 + bcol, ldb2);
    issue_w(rB, st + 16384 + 8192, lw, vb, (unsigned)kt * 64u * ldb2 + bcol + 128u, ldb2);
  }
};
template <int TS_> struct FlagHook {
  static constexpr int TS = TS_;
  brs_t r; unsigned off; bool me;
  DI void pre() const {}
  DI void post() const { if (me) __builtin_amdgcn_raw_buffer_store_b32(1u, r, (int)off, 0, 16); }
};
DI void await_flags(unsigned v, const char* fp) {
  if (__builtin_amdgcn_ballot_w64(v == 0u) == 0ull) return;
  for (int it = 0; it < (1 << 18); ++it) {
    asm volatile("global_load_dword %0, %1, off sc1\n\ts_waitcnt vmcnt(0)" : "=v"(v) : "v"(fp) : "memory");
    if (__builtin_amdgcn_ballot_w64(v == 0u) == 0ull) break;
    __builtin_amdgcn_s_sleep(1);
  }
}
__global__ __launch_bounds__(512, 2) void k2(const u16* __restrict__ x, const u16* __restrict__ wqkv, const u16* __restrict__ w2,
                                             const u16* __restrict__ w3, char* __restrict__ ws) {
  extern __shared__ __attribute__((aligned(16))) char smem_[];
  lds_t smem = (lds_t)smem_;
  typedef C2 C;
  STAMP0();
  const int w = __builtin_amdgcn_readfirstlane(threadIdx.x >> 6), lane = threadIdx.x & 63;
  const int xcd = blockIdx.x & 7, j = blockIdx.x >> 3;
  const bool isq = xcd < 4;
  const int pm = (xcd & 1) * 8 + (j & 7), pn = ((xcd >> 1) & 1) * 4 + (j >> 3);
  const int fi = lane & 15, g = lane >> 4;
  const brs_t rW = mk_brs(ws, WS_BYTES);
  lds_t X = smem + C::XTRA;
  if (w >= 4) {
    const int lw = w - 4;
    const unsigned l16 = (unsigned)lane * 16u;
    L2 L;
    L.rA = mk_rsrc(isq ? (const void*)x : (const void*)(ws + WS_HS), S_ * D_ * 2);
    L.rB = isq ? mk_rsrc(wqkv, D_ * 3 * D_ * 2) : mk_rsrc(w2, D_ * D_ * 2);
    L.lw = lw; L.ldb2 = isq ? 6144u : 2048u; L.smem = smem;
    L.va = voff_a(lane, lw, 2048u); L.vb = voff_w(lane, lw, L.ldb2);
    L.aoff = (unsigned)pm * 128u * 2048u; L.bcol = (unsigned)pn * 256u;
    const rsrc_t rS = mk_rsrc(ws, WS_BYTES);
    const int kh = blockIdx.x >> 3, kTU = (blockIdx.x >> 1) & 3, khalf = blockIdx.x & 1;
    LST(c1_);
    L.issue(0);
    LST(c5_);
    L.issue(1);
#pragma unroll
    for (int i = 0; i < 4; ++i)
      glds16(rS, X + (lw * 4 + i) * 1024, (unsigned)(lane >> 5) * 4096u + (lane & 31) * 16u, (unsigned)WS_KVP + ((kh * 32 + lw * 8 + 2 * i) * 4 + kTU) * 1024u + khalf * 512u);
    LST(c6_);
    L.issue(2);
    LST(c2_);
    wait_vm<20>();
    LST(c3_);
    bar();
    L.issue(3); wait_vm<16>(); bar();
    L.issue(4);
    {
      lds_t p = X + (lane >> 3) * 512 + (lw * 8 + (lane & 7)) * 16;
      f32x4 s = *(const LAS f32x4*)p;
#pragma unroll
      for (int i = 1; i < 4; ++i) s += *(const LAS f32x4*)(p + i * 4096);
#pragma unroll
      for (int e = 0; e < 4; ++e) s[e] = sum_r8(s[e]);
      u32x2 v; v.x = cvt_pk(s[0], s[1]); v.y = cvt_pk(s[2], s[3]);
      if (lane < 8) st8(rW, (unsigned)WS_KVF + (kh * 2 + (kTU & 1)) * 1024 + (khalf * 32 + lw * 8 + lane) * 16 + (kTU >> 1) * 8, v);
    }
    wait_vm<17>(); bar();
    L.issue(5); wait_vm<17>(); bar();
    L.issue(6); wait_vm<17>(); bar();
    for (int t = 7; t < 14; ++t) { L.issue(t); wait_vm<16>(); bar(); }
    L.issue(14);
    const unsigned foff = (unsigned)WS_FLAG + 65536u + pn * 128u + (lane & 31) * 4u;
    gpoll(rS, X + 28672 + lw * 256, foff);
    wait_vm<17>(); bar();
    L.issue(15); wait_vm<17>(); bar();
    wait_vm<8>();
    const int u = blockIdx.x < 224 ? blockIdx.x : blockIdx.x - 224, slab = u % 56, rg = (u / 56) * 4 + lw;
    const rsrc_t rN = mk_rsrc(w3, D_ * F_ * 2);
    const unsigned noff = (unsigned)rg * (64u * 7168u) + slab * 128u, nv = (unsigned)(lane >> 3) * 7168u + (lane & 7) * 16u;
    if (isq) {
      await_flags(*(const LAS unsigned*)(X + 28672 + lw * 256 + lane * 4), ws + foff);
      const int head = pn * 4 + lw;
#pragma unroll
      for (int i = 0; i < 2; ++i) glds16c(rS, X + (lw * 2 + i) * 1024, l16, (unsigned)WS_KVF + (head * 2 + i) * 1024u);
#pragma unroll
      for (int i = 0; i < 4; ++i) glds16(rS, X + 8192 + (lw * 4 + i) * 1024, l16, (unsigned)WS_ZP + head * 4096u + i * 1024u);
      glds16(rS, X + 24576, l16, (unsigned)WS_G + pm * 1024u);
      bar();
#pragma unroll
      for (int i = 0; i < 8; ++i) glds16(rN, smem + (lw * 8 + i) * 1024, nv, noff + i * (8u * 7168u));
      wait_vm<15>(); bar();
      wait_vm<8>();
      f32x4 z = *(const LAS f32x4*)(X + 8192 + lw * 4096 + l16);
#pragma unroll
      for (int i = 1; i < 4; ++i) z += *(const LAS f32x4*)(X + 8192 + lw * 4096 + i * 1024 + l16);
#pragma unroll
      for (int e = 0; e < 4; ++e) z[e] = sum_r8(z[e]);
      u32x2 v; v.x = cvt_pk(z[0], z[1]); v.y = cvt_pk(z[2], z[3]);
      if (lane < 8) *(LAS u32x2*)(X + 25600 + lw * 64 + (lane & 3) * 16 + (lane >> 2) * 8) = v;
      wait_lgkm0();
    } else {
      const rsrc_t rX = mk_rsrc(x, S_ * D_ * 2);
#pragma unroll
      for (int i = 0; i < 8; ++i) {
        const int r = (lw * 8 + i) * 4;
        glds16(rX, X + r * 256, (unsigned)g * 2048u + (unsigned)((fi ^ ((r + g) & 15)) * 16), (unsigned)(pm * 128 + r) * 2048u + pn * 256u);
      }
      bar();
#pragma unroll
      for (int i = 0; i < 8; ++i) glds16(rN, smem + (lw * 8 + i) * 1024, nv, noff + i * (8u * 7168u));
      wait_vm<16>(); bar();
      wait_vm<8>();
    }
    bar();
    wait_vm<0>();
    float c[8];
#pragma unroll
    for (int e = 0; e < 8; ++e) c[e] = 0.f;
#pragma unroll
    for (int i = 0; i < 8; ++i) {
      const u32x4 v = *(const LAS u32x4*)(smem + (lw * 8 + i) * 1024 + lane * 16);
#pragma unroll
      for (int e = 0; e < 4; ++e) { c[2 * e] += bf_lo(v[e]); c[2 * e + 1] += bf_hi(v[e]); }
    }
#pragma unroll
    for (int e = 0; e < 8; ++e) c[e] = sum_r8(c[e]);
    if (lane < 8 && blockIdx.x < 224) {
      const unsigned o = (unsigned)WS_CS3 + (rg * F_ + slab * 64 + lane * 8) * 4;
      st16f(rW, o, (f32x4){c[0], c[1], c[2], c[3]}); st16f(rW, o + 16, (f32x4){c[4], c[5], c[6], c[7]});
    }
    LST_OUT(1);
    STAMP_END(1, 2);
    return;
  }
  const int wm = w >> 1, wn = w & 1;
  unsigned boff[4];
#pragma unroll
  for (int i = 0; i < 4; ++i) boff[i] = 16384 + wn * 8192 + roff_w(lane, i);
  f32x4 acc[4][4];
  FlagHook<4> fh; fh.r = rW; fh.off = (unsigned)WS_FLAG + 65536u + blockIdx.x * 4u; fh.me = threadIdx.x == 0;
  consume<C, P2>(smem, 16, acc, wm, lane, boff, fh CARG(1));
  STAMP(1, 1); STAMPC(1);
  bar();
  const int row0 = pm * 128 + wm * 64, col0 = pn * 128 + wn * 64 + quad_col(lane);
  if (isq) {
    bf16x8 kf[2][2], zf[2];
#pragma unroll
    for (int h = 0; h < 2; ++h) {
      kf[h][0] = *(const LAS bf16x8*)(X + (wn * 2 + h) * 2048 + lane * 16); kf[h][1] = *(const LAS bf16x8*)(X + (wn * 2 + h) * 2048 + 1024 + lane * 16);
      zf[h] = *(const LAS bf16x8*)(X + 25600 + (wn * 2 + h) * 64 + g * 16);
    }
#pragma unroll
    for (int m = 0; m < 4; ++m) {
      const float gg = *(const LAS float*)(X + 24576 + (wm * 64 + m * 16 + fi) * 8);
      unsigned p[4][2];
#pragma unroll
      for (int h = 0; h < 2; ++h) {
        const f32x4 a = acc[m][2 * h], b = acc[m][2 * h + 1], zero = {0.f, 0.f, 0.f, 0.f};
        u32x4 q; q.x = cvt_pk(phi_f(a[0]), phi_f(a[1])); q.y = cvt_pk(phi_f(a[2]), phi_f(a[3])); q.z = cvt_pk(phi_f(b[0]), phi_f(b[1])); q.w = cvt_pk(phi_f(b[2]), phi_f(b[3]));
        const bf16x8 pq = __builtin_bit_cast(bf16x8, q);
        const f32x4 n0 = __builtin_amdgcn_mfma_f32_16x16x32_bf16(kf[h][0], pq, zero, 0, 0, 0);
        const f32x4 n1 = __builtin_amdgcn_mfma_f32_16x16x32_bf16(kf[h][1], pq, zero, 0, 0, 0);
        const f32x4 dn = __builtin_amdgcn_mfma_f32_16x16x32_bf16(zf[h], pq, zero, 0, 0, 0);
        const float sc = gg / (dn[0] + 1e-6f);
        p[2 * h][0] = cvt_pk(n0[0] * sc, n0[1] * sc); p[2 * h][1] = cvt_pk(n0[2] * sc, n0[3] * sc);
        p[2 * h + 1][0] = cvt_pk(n1[0] * sc, n1[1] * sc); p[2 * h + 1][1] = cvt_pk(n1[2] * sc, n1[3] * sc);
      }
      store_quad(rW, (unsigned)WS_AU + ((row0 + m * 16 + (fi & 7)) * LDAU + col0) * 2, 8 * LDAU * 2, p);
    }
  } else {
    lds_t xl = X + (wm * 64 + fi) * 256 + (g & 1) * 8;
#pragma unroll
    for (int m = 0; m < 4; ++m) {
      float s1 = 0.f, s2 = 0.f;
      unsigned p[4][2];
#pragma unroll
      for (int n = 0; n < 4; ++n) {
        const u32x2 xr = *(const LAS u32x2*)(xl + m * 4096 + (((wn * 8 + n * 2 + (g >> 1)) ^ fi) * 16));
        p[n][0] = cvt_pk(acc[m][n][0] + bf_lo(xr.x), acc[m][n][1] + bf_hi(xr.x));
        p[n][1] = cvt_pk(acc[m][n][2] + bf_lo(xr.y), acc[m][n][3] + bf_hi(xr.y));
        s1 = dot2(p[n][0], ONES, s1); s1 = dot2(p[n][1], ONES, s1);
        s2 = dot2(p[n][0], p[n][0], s2); s2 = dot2(p[n][1], p[n][1], s2);
      }
      store_quad(rW, (unsigned)WS_X1 + ((row0 + m * 16 + (fi & 7)) * D_ + col0) * 2, 16384, p);
      s1 = sum_g(s1); s2 = sum_g(s2);
      if (g == 0) { u32x2 v; v.x = __float_as_uint(s1); v.y = __float_as_uint(s2); st8(rW, (unsigned)WS_ST + (row0 + m * 16 + fi) * 128 + (pn * 2 + wn) * 8, v); }
    }
  }
  STAMP_END(1, 2);
}

typedef Cfg<3, 1, 7, 3> C3;
struct P3 {
  static constexpr int NBASE = 7;
  static constexpr bool trans(int) { return true; }
  static constexpr bool narrow(int i) { return i == 6; }
  static constexpr int base(int i) { return i; }
  static constexpr int imm(int) { return 0; }
};
struct L3 {
  static constexpr int NTAIL = 11;
  u64 c_[5];
  rsrc_t rA, rB, rS, rO, rF; unsigned va, vb, vn, aoff, wcol, ncol; int lw, lane, pm, pn; lds_t xtra;
  DI void issue(lds_t st, int kt) {
    issue_a(rA, st, lw, va, aoff + (unsigned)kt * 128u, 2048u);
    const unsigned kb = (unsigned)kt * (64u * 7168u);
#pragma unroll
    for (int b = 0; b < 3; ++b) issue_w(rB, st + 16384 + b * 8192, lw, vb, kb + wcol + b * 128u, 7168u);
    glds16(rB, st + 16384 + 24576 + lw * 1024, vn, kb + ncol + (unsigned)lw * (16u * 7168u));
  }
  DI void tail(lds_t st) {
    st += lw * 9216;
    const unsigned l16 = (unsigned)lane * 16u;
#pragma unroll
    for (int i = 0; i < 4; ++i) glds16(rS, st + i * 1024, l16, (unsigned)WS_ST + (pm * 128 + lw * 32) * 128u + i * 1024u);
#pragma unroll
    for (int i = 0; i < 4; ++i) glds16(rS, st + 4096 + i * 1024, l16, (unsigned)WS_CS3 + ((lw * 4 + i) * F_ + pn * 224) * 4u);
    glds16(rS, st + 8192, l16, (unsigned)WS_G + pm * 1024u);
    const unsigned lo = (unsigned)((blockIdx.x * 4 + lw) * 16 + (lane & 15)), lf = (unsigned)((blockIdx.x * 4 + lw) * 56 + (lane < 56 ? lane : 55));
    gtouch(rF, xtra + 6144, lf * 128u, 0u);
    gtouch(rO, xtra + 6144, lo * 128u, 0u);
  }
};
typedef Cfg<2, 0, 4, 4> C4;
struct L4 {
  rsrc_t rA, rO, rW; unsigned va, vb, aoff, bcol; int lw, hf, own; lds_t smem;
  DI void issue(int t) {
    lds_t st = smem + (t & 3) * C4::STAGE;
    const bool o = t < 8;
    const int r = t - 11, kb = o ? hf * 8 + t : hf * 28 + (t < 11 ? own + t - 8 : r + (r >= own ? 3 : 0));
    const unsigned so = aoff + (unsigned)(o ? kb : kb + 16) * 128u;
#pragma unroll
    for (int i = 0; i < 4; ++i) glds16c(rA, st + (lw + 4 * i) * 1024, va, so + (unsigned)(lw + 4 * i) * (8u * LDAU * 2u));
    rsrc_t rB; rB.x = o ? rO.x : rW.x; rB.y = o ? rO.y : rW.y; rB.z = o ? rO.z : rW.z; rB.w = rO.w;
    issue_w(rB, st + 16384, lw, vb, (unsigned)kb * (64u * 2048u) + bcol, 2048u);
    issue_w(rB, st + 16384 + 8192, lw, vb, (unsigned)kb * (64u * 2048u) + bcol + 128u, 2048u);
  }
};
#define TSU 4
#define TPOLL 10
struct DrainHook : FlagHook<TSU> { DI void pre() const { wait_vm<0>(); } };
__global__ __launch_bounds__(512, 2) void k3(const u16* __restrict__ w3, const u16* __restrict__ wo, const u16* __restrict__ w4,
                                             char* __restrict__ ws, u16* __restrict__ out) {
  extern __shared__ __attribute__((aligned(16))) char smem_[];
  lds_t smem = (lds_t)smem_;
  const int w = __builtin_amdgcn_readfirstlane(threadIdx.x >> 6), lane = threadIdx.x & 63;
  const int xcd = blockIdx.x & 7, j = blockIdx.x >> 3;
  const int pm = (xcd & 1) * 8 + (j & 7), pn = (xcd >> 1) * 4 + (j >> 3);
  const int hf = pn >> 3, pn4 = pn & 7;
  const int fi = lane & 15, g = lane >> 4;
  const brs_t rW = mk_brs(ws, WS_BYTES);
  const int wc0 = (pn & 1) ? 32 : 0, nc0 = (pn & 1) ? 0 : 192;
  LAS f32x4* rst = (LAS f32x4*)(smem + C3::XTRA);
  LAS float* xcs = (LAS float*)(smem + C3::XTRA + 2048);
  constexpr unsigned UFLAG = (unsigned)WS_FLAG + 65536u + 1024u;
  if (w >= 4) {
    const int lw = w - 4;
    const rsrc_t rS = mk_rsrc(ws, WS_BYTES), rO = mk_rsrc(wo, D_ * D_ * 2), rF = mk_rsrc(w4, F_ * D_ * 2);
    {
      typedef C3 C;
      STAMP0();
      L3 L;
      L.rA = mk_rsrc(ws + WS_X1, S_ * D_ * 2); L.rB = mk_rsrc(w3, D_ * F_ * 2); L.rS = rS; L.rO = rO; L.rF = rF;
      L.lw = lw; L.lane = lane; L.pm = pm; L.pn = pn; L.xtra = smem + C::XTRA;
      L.va = voff_a(lane, lw, 2048u); L.vb = voff_w(lane, lw, 7168u); L.vn = voff_n(lane, 7168u);
      L.aoff = (unsigned)pm * 128u * 2048u; L.wcol = (unsigned)(pn * 224 + wc0) * 2u; L.ncol = (unsigned)(pn * 224 + nc0) * 2u;
      produce<C>(smem, 16, L CARG(2));
      wait_vm<2>();
      lds_t st = smem + C::STAGE + lw * 9216;
      f32x4 s = *(const LAS f32x4*)(st + lane * 64), c = *(const LAS f32x4*)(st + 4096 + lane * 16);
#pragma unroll
      for (int i = 1; i < 4; ++i) { s += *(const LAS f32x4*)(st + lane * 64 + i * 16); c += *(const LAS f32x4*)(st + 4096 + i * 1024 + lane * 16); }
      const int lr = lw * 32 + (lane >> 1);
      const float omg = *(const LAS float*)(st + 8192 + lr * 8 + 4);
      float s1 = s[0] + s[2], s2 = s[1] + s[3];
      s1 += __shfl_xor(s1, 1); s2 += __shfl_xor(s2, 1);
      const float mean = s1 * (1.f / 1024.f);
      const float var = fmaxf(s2 * (1.f / 1024.f) - mean * mean, 0.f);
      const float rstd = 1.f / sqrtf(var + 1e-5f);
      if (!(lane & 1)) rst[lr] = (f32x4){-mean, -2.30220819814f * rstd, -0.10294323970f * rstd * rstd * rstd, fminf(1.f / (rstd * omg), 1e30f)};
      *(LAS f32x4*)(xcs + lw * 256 + lane * 4) = c;
      wait_lgkm0();
      bar();
      LST_OUT2(2, L);
      STAMP_NW(2, 2);
    }
    STAMP0();
    L4 L;
    L.rA = mk_rsrc(ws + WS_AU, S_ * LDAU * 2); L.rO = rO; L.rW = rF; L.smem = smem;
    L.lw = lw; L.hf = hf; L.own = (7 * pn4 + 1) >> 1; L.va = voff_a(lane, lw, LDAU * 2u); L.vb = voff_w(lane, lw, 2048u);
    L.aoff = (unsigned)pm * 128u * (LDAU * 2u); L.bcol = (unsigned)pn4 * 256u;
    LST(c1_); L.issue(0); LST(c5_); L.issue(1); LST(c6_); L.issue(2); LST(c2_);
    wait_vm<16>();
    LST(c3_);
    bar();
    int t = 3;
#pragma unroll 1
    for (; t < TPOLL; ++t) { L.issue(t); wait_vm<16>(); bar(); }
    const unsigned foff = UFLAG + (pm * 16 + hf * 8 + (lane & 7)) * 4u;
    lds_t fl = smem + C3::XTRA + 6144 + 256 + lw * 256;
    gpoll(rS, fl, foff);
#pragma unroll 1
    for (; t < 11; ++t) { L.issue(t); wait_vm<16>(); bar(); }
    wait_vm<8 * (11 - TPOLL)>();
    await_flags(*(const LAS unsigned*)(fl + lane * 4), ws + foff);
#pragma unroll 1
    for (; t < 36; ++t) { L.issue(t); wait_vm<16>(); bar(); }
    wait_vm<8>(); bar();
    wait_vm<0>(); bar();
    LST_OUT(3);
    STAMP_END(3, 2);
    return;
  }
  const int wm = w >> 1, wn = w & 1;
  {
    typedef C3 C;
    STAMP0();
    unsigned boff[7];
#pragma unroll
    for (int i = 0; i < 4; ++i) boff[i] = 16384 + wn * 8192 + roff_w(lane, i);
    boff[4] = 16384 + 16384 + roff_w(lane, 2 * wn); boff[5] = 16384 + 16384 + roff_w(lane, 2 * wn + 1);
    boff[6] = 16384 + 24576 + roff_n(lane, wn);
    f32x4 acc[4][7];
    consume<C, P3>(smem, 16, acc, wm, lane, boff, NoHook() CARG(2));
    STAMP(2, 1); STAMPC(2);
    bar();
    f32x4 cs[7];
#pragma unroll
    for (int n = 0; n < 7; ++n) {
      const int c0 = (n < 4 ? wc0 + wn * 64 + n * 16 : n < 6 ? wc0 + 128 + wn * 32 + (n - 4) * 16 : nc0 + wn * 16) + g * 4;
      cs[n] = *(const LAS f32x4*)(xcs + c0);
#pragma unroll
      for (int i = 1; i < 4; ++i) cs[n] += *(const LAS f32x4*)(xcs + i * 256 + c0);
    }
    const unsigned urow = (unsigned)WS_AU + ((pm * 128 + wm * 64) * LDAU + 1024 + pn * 224) * 2;
    unsigned q6[2];
#pragma unroll
    for (int m = 0; m < 4; ++m) {
      const f32x4 rs = rst[wm * 64 + m * 16 + fi];
      const f32x2 nm = {rs[0], rs[0]}, c0 = {rs[1], rs[1]}, c1 = {rs[2], rs[2]}, q = {rs[3], rs[3]};
      unsigned p[7][2];
#pragma unroll
      for (int n = 0; n < 7; ++n)
#pragma unroll
        for (int h = 0; h < 2; ++h) {
          const f32x2 a = {acc[m][n][2 * h], acc[m][n][2 * h + 1]}, c = {cs[n][2 * h], cs[n][2 * h + 1]};
          const f32x2 t = __builtin_elementwise_fma(c, nm, a);
          const f32x2 y = t * __builtin_elementwise_fma(t * t, c1, c0);
          const f32x2 e = {__builtin_amdgcn_exp2f(y[0]), __builtin_amdgcn_exp2f(y[1])};
          const f32x2 d = __builtin_elementwise_fma(e, q, q);
          const f32x2 o = t * (f32x2){__builtin_amdgcn_rcpf(d[0]), __builtin_amdgcn_rcpf(d[1])};
          p[n][h] = cvt_pk(o[0], o[1]);
        }
      const unsigned pq[4][2] = {{p[0][0], p[0][1]}, {p[1][0], p[1][1]}, {p[2][0], p[2][1]}, {p[3][0], p[3][1]}};
      store_quad(rW, urow + ((m * 16 + (fi & 7)) * LDAU + wc0 + wn * 64 + quad_col(lane)) * 2, 8 * LDAU * 2, pq);
      st16(rW, urow + ((m * 16 + fi) * LDAU + wc0 + 128 + wn * 32 + (g & 1) * 16 + (g & 2) * 4) * 2, pair_swap(p[4][0], p[4][1], p[5][0], p[5][1]));
      if (m & 1) st16(rW, urow + (((m - 1 + (g & 1)) * 16 + fi) * LDAU + nc0 + wn * 16 + (g & 2) * 4) * 2, pair_swap(q6[0], q6[1], p[6][0], p[6][1]));
      else { q6[0] = p[6][0]; q6[1] = p[6][1]; }
    }
    STAMP_NW(2, 2);
  }
  typedef C4 C;
  STAMP0();
  const int row0 = pm * 128 + wm * 64 + hf * 32;
  const unsigned eoff = (unsigned)((row0 + (fi & 7)) * D_ + pn4 * 128 + wn * 64 + quad_col(lane)) * 2;
  u32x4 xr[2][2]; float omg[2];
#pragma unroll
  for (int m = 0; m < 2; ++m) {
#pragma unroll
    for (int p = 0; p < 2; ++p) xr[m][p] = *(const u32x4*)(ws + WS_X1 + eoff + (m * 16 + p * 8) * 2048);
    omg[m] = *(const float*)(ws + WS_G + (size_t)(row0 + m * 16 + fi) * 8 + 4);
  }
  unsigned boff[4];
#pragma unroll
  for (int i = 0; i < 4; ++i) boff[i] = 16384 + wn * 8192 + roff_w(lane, i);
  f32x4 acc[4][4];
  DrainHook dh; dh.r = rW; dh.off = UFLAG + (pm * 16 + pn) * 4u; dh.me = threadIdx.x == 0;
  consume<C, P2>(smem, 36, acc, wm, lane, boff, dh CARG(3));
  STAMP(3, 1); STAMPC(3);
  f32x4 own[2][4];
  {
    const unsigned ob = (unsigned)WS_XCH + (blockIdx.x * 4 + w) * 8192 + lane * 16;
#pragma unroll
    for (int m = 0; m < 2; ++m)
#pragma unroll
      for (int n = 0; n < 4; ++n) {
        f32x4 snd;
#pragma unroll
        for (int r = 0; r < 4; ++r) { snd[r] = hf ? acc[m][n][r] : acc[2 + m][n][r]; own[m][n][r] = hf ? acc[2 + m][n][r] : acc[m][n][r]; }
        st16f(rW, ob + (m * 4 + n) * 1024, snd);
      }
    wait_vm<0>();
    STAMP(3, 3);
    bar();
    if (threadIdx.x == 0) __builtin_amdgcn_raw_buffer_store_b32(1u, rW, (int)((unsigned)WS_FLAG + blockIdx.x * 128), 0, 16);
  }
  {
    await_flags(0u, ws + WS_FLAG + (size_t)(blockIdx.x ^ 4) * 128);
    STAMP(3, 4);
    const unsigned ib = (unsigned)WS_XCH + ((blockIdx.x ^ 4) * 4 + w) * 8192 + lane * 16;
    f32x4 o[2][4];
#pragma unroll
    for (int m = 0; m < 2; ++m)
#pragma unroll
      for (int n = 0; n < 4; ++n) o[m][n] = __builtin_bit_cast(f32x4, __builtin_amdgcn_raw_buffer_load_b128(rW, (int)(ib + (m * 4 + n) * 1024), 0, 16));
    const brs_t rO = mk_brs(out, S_ * D_ * 2);
#pragma unroll
    for (int m = 0; m < 2; ++m) {
      unsigned p[4][2];
      unpack_quad(xr[m][0], xr[m][1], p);
      const float q = omg[m];
#pragma unroll
      for (int n = 0; n < 4; ++n) {
        const f32x4 a = own[m][n] + o[m][n];
        p[n][0] = cvt_pk(__builtin_fmaf(q, bf_lo(p[n][0]), a[0]), __builtin_fmaf(q, bf_hi(p[n][0]), a[1]));
        p[n][1] = cvt_pk(__builtin_fmaf(q, bf_lo(p[n][1]), a[2]), __builtin_fmaf(q, bf_hi(p[n][1]), a[3]));
      }
      store_quad(rO, eoff + m * 32768, 16384, p);
    }
  }
  STAMP_END(3, 2);
}

extern "C" __global__ void kend() {}

extern "C" void kernel_launch(void* const* d_in, const int* in_sizes, int n_in, void* d_out, int out_size, void* d_ws, size_t ws_size,
                              hipStream_t stream) {
  const u16* x = (const u16*)d_in[0];
  const u16* wqkv = (const u16*)d_in[1];
  const u16* wo = (const u16*)d_in[2];
  const u16* w1 = (const u16*)d_in[4];
  const u16* w2 = (const u16*)d_in[5];
  const u16* w3 = (const u16*)d_in[6];
  const u16* w4 = (const u16*)d_in[7];
  const u16* gw = (const u16*)d_in[8];
  char* ws = (char*)d_ws + WSOFF;
  (void)hipFuncSetAttribute((const void*)k1, hipFuncAttributeMaxDynamicSharedMemorySize, C1::LDS);
  (void)hipFuncSetAttribute((const void*)k2, hipFuncAttributeMaxDynamicSharedMemorySize, LDS2);
  (void)hipFuncSetAttribute((const void*)k3, hipFuncAttributeMaxDynamicSharedMemorySize, C3::LDS);
  k0<<<256, 512, 0, stream>>>(x, wqkv, w1, gw, ws);
  k1<<<256, 512, C1::LDS, stream>>>(x, wqkv, w1, w2, ws);
  k2<<<256, 512, LDS2, stream>>>(x, wqkv, w2, w3, ws);
  k3<<<256, 512, C3::LDS, stream>>>(w3, wo, w4, ws, (u16*)d_out);
}
```

```cpp
#include <hip/hip_runtime.h>
#define CAL 64
#define ALN [[clang::code_align(CAL)]]

typedef unsigned short u16;
typedef unsigned long long u64;
typedef short bf16x8 __attribute__((ext_vector_type(8)));
typedef short s16x4 __attribute__((ext_vector_type(4)));
typedef float f32x4 __attribute__((ext_vector_type(4)));
typedef float f32x2 __attribute__((ext_vector_type(2)));
typedef unsigned u32x4 __attribute__((ext_vector_type(4)));
typedef unsigned u32x2 __attribute__((ext_vector_type(2)));
typedef __bf16 bf16x2_t __attribute__((ext_vector_type(2)));
#define LAS __attribute__((address_space(3)))
typedef LAS char* lds_t;
typedef u32x4 rsrc_t;
#define DI __device__ __forceinline__

constexpr int S_ = 2048, D_ = 1024, F_ = 3584;
constexpr int LDAU = 4608 + 64;
#define SLOWCP 1
#define LDH 2176u
constexpr size_t MB = 1u << 20;
constexpr size_t WS_HS = 0, WS_X1 = 6 * MB, WS_AU = 12 * MB, WS_KVP = 32 * MB, WS_ZP = 36 * MB, WS_KVF = 36 * MB + 512 * 1024,
                 WS_ST = 37 * MB, WS_RS = 37 * MB + 512 * 1024, WS_FLAG = 38 * MB, WS_EPOCH = 38 * MB + 512 * 1024, WS_CS3 = 39 * MB,
                 WS_XCH = 40 * MB, WS_CPA = 4 * MB + 512 * 1024, WS_CPE = 10 * MB + 512 * 1024;
constexpr unsigned FLG(int kind) { return (unsigned)WS_FLAG + kind * 4096u; }
enum { F_RS = 0, F_HS = 1, F_X1 = 2, F_KVP = 3, F_KVF = 4, F_OG = 5, F_U = 6, F_XCH = 7, F_CS3 = 8, F_U1 = 9 };

DI rsrc_t mk_rsrc(const void* p, unsigned bytes) {
  u64 a = (u64)p; rsrc_t r; r.x = (unsigned)a; r.y = (unsigned)(a >> 32) & 0xffffu; r.z = bytes; r.w = 0x00020000u; return r;
}
template <bool SC> DI void glds16(rsrc_t r, lds_t dst, unsigned voff, unsigned soff) {
  if (SC) asm volatile("s_mov_b32 m0, %0\n\ts_nop 0\n\tbuffer_load_dwordx4 %1, %2, %3 offen sc1 lds" ::"s"((unsigned)(size_t)dst), "v"(voff), "s"(r), "s"(soff) : "memory");
  else asm volatile("s_mov_b32 m0, %0\n\ts_nop 0\n\tbuffer_load_dwordx4 %1, %2, %3 offen lds" ::"s"((unsigned)(size_t)dst), "v"(voff), "s"(r), "s"(soff) : "memory");
}
template <bool SC = false> DI void gtouch(rsrc_t r, lds_t dst, unsigned voff) {
  if (SC) asm volatile("s_mov_b32 m0, %0\n\ts_nop 0\n\tbuffer_load_dword %1, %2, 0 offen sc1 lds" ::"s"((unsigned)(size_t)dst), "v"(voff), "s"(r) : "memory");
  else asm volatile("s_mov_b32 m0, %0\n\ts_nop 0\n\tbuffer_load_dword %1, %2, 0 offen lds" ::"s"((unsigned)(size_t)dst), "v"(voff), "s"(r) : "memory");
}
template <int N> DI void wait_vm() { asm volatile("s_waitcnt vmcnt(%0)" ::"n"(N) : "memory"); }
DI void wait_lgkm0() { asm volatile("" ::: "memory"); __builtin_amdgcn_s_waitcnt(0xC07F); asm volatile("" ::: "memory"); }
DI void bar() { asm volatile("" ::: "memory"); __builtin_amdgcn_s_barrier(); asm volatile("" ::: "memory"); }
DI unsigned cvt_pk(float lo, float hi) { f32x2 v = {lo, hi}; bf16x2_t r = __builtin_convertvector(v, bf16x2_t); return __builtin_bit_cast(unsigned, r); }
DI float bf_lo(unsigned u) { return __builtin_bit_cast(float, u << 16); }
DI float bf_hi(unsigned u) { return __builtin_bit_cast(float, u & 0xffff0000u); }
DI float rbf(float f) { return bf_lo(cvt_pk(f, 0.f) & 0xffffu); }
DI float dot2(unsigned a, unsigned b, float c) { return __builtin_amdgcn_fdot2_f32_bf16(__builtin_bit_cast(bf16x2_t, a), __builtin_bit_cast(bf16x2_t, b), c, false); }
constexpr unsigned ONES = 0x3f803f80u;
typedef __amdgpu_buffer_rsrc_t brs_t;
constexpr unsigned WS_BYTES = 48u << 20;
DI brs_t mk_brs(void* p, unsigned bytes) { return __builtin_amdgcn_make_buffer_rsrc(p, (short)0, (int)bytes, 0x00020000); }
DI void st16(brs_t r, unsigned off, u32x4 v) { __builtin_amdgcn_raw_buffer_store_b128(v, r, (int)off, 0, 16); }
DI void st8(brs_t r, unsigned off, u32x2 v) { __builtin_amdgcn_raw_buffer_store_b64(v, r, (int)off, 0, 16); }
DI void st4(brs_t r, unsigned off, unsigned v) { __builtin_amdgcn_raw_buffer_store_b32(v, r, (int)off, 0, 16); }
DI void st16f(brs_t r, unsigned off, f32x4 v) { __builtin_amdgcn_raw_buffer_store_b128(__builtin_bit_cast(u32x4, v), r, (int)off, 0, 16); }
DI u32x4 ld16(brs_t r, unsigned off) { return __builtin_amdgcn_raw_buffer_load_b128(r, (int)off, 0, 16); }
#define CODE_TOUCH(B, E, i, t_) do { u64 pc_; unsigned lo_, len_; asm volatile("s_getpc_b64 %0\n1:\n\ts_mov_b32 %1, 1b-" B "\n\ts_mov_b32 %2, " E "-" B : "=s"(pc_), "=s"(lo_), "=s"(len_)); \
    pc_ -= lo_; const unsigned o_ = (unsigned)(i) * 128u; if (o_ < len_) asm volatile("global_load_dword %0, %1, %2" : "+v"(t_) : "v"(o_), "s"(pc_) : "memory"); } while (0)
DI float sum_p16(float v) {
  const unsigned u = __builtin_bit_cast(unsigned, v);
  auto a = __builtin_amdgcn_permlane16_swap(u, u, false, false);
  const unsigned a0 = a[0], a1 = a[1];
  return __uint_as_float(a0) + __uint_as_float(a1);
}
DI float sum_g(float v) {
  const unsigned u = __builtin_bit_cast(unsigned, sum_p16(v));
  auto b = __builtin_amdgcn_permlane32_swap(u, u, false, false);
  const unsigned b0 = b[0], b1 = b[1];
  return __uint_as_float(b0) + __uint_as_float(b1);
}
template <int CTL> DI float add_dpp(float v) { return v + __int_as_float(__builtin_amdgcn_update_dpp(0, __float_as_int(v), CTL, 0xf, 0xf, false)); }
DI float sum_r8(float v) { return sum_g(add_dpp<0x128>(v)); }
DI float sum_h32(float v) { return sum_p16(add_dpp<0x121>(add_dpp<0x122>(add_dpp<0x124>(add_dpp<0x128>(v))))); }
DI float gelu_t(float v) {
  const float e = __builtin_amdgcn_exp2f(v * __builtin_fmaf(v * v, -0.10294323970f, -2.30220819814f));
  return v * __builtin_amdgcn_rcpf(1.f + e);
}
DI float phi_f(float t) {
  return (__int_as_float(max(__float_as_int(t), 0)) + __builtin_amdgcn_fmed3f(__builtin_amdgcn_exp2f(t * 1.44269504089f), 0.f, 1.f)) + 1e-6f;
}
DI unsigned phi2(float t0, float t1) { return cvt_pk(phi_f(t0), phi_f(t1)); }
DI u32x4 phi_pk(const f32x4& a, const f32x4& b) {
  u32x4 q; q.x = phi2(a[0], a[1]); q.y = phi2(a[2], a[3]); q.z = phi2(b[0], b[1]); q.w = phi2(b[2], b[3]); return q;
}
DI void swap16(unsigned& x, unsigned& y) { auto r = __builtin_amdgcn_permlane16_swap(x, y, false, false); const unsigned r0 = r[0], r1 = r[1]; x = r0; y = r1; }
DI void swap32(unsigned& x, unsigned& y) { auto r = __builtin_amdgcn_permlane32_swap(x, y, false, false); const unsigned r0 = r[0], r1 = r[1]; x = r0; y = r1; }
DI void tile_xpose(unsigned (&p)[4][2]) {
#pragma unroll
  for (int e = 0; e < 2; ++e) { swap32(p[0][e], p[2][e]); swap32(p[1][e], p[3][e]); swap16(p[0][e], p[1][e]); swap16(p[2][e], p[3][e]); }
}
DI void par_xchg(u32x4& a, u32x4& b, bool odd) {
  u32x4 c, d;
#define CSEL(D, S, K) "\n\tv_cndmask_b32_dpp " D ", " S ", " K ", vcc quad_perm:[1,0,3,2] row_mask:0xf bank_mask:0xf"
  asm("s_mov_b64 vcc, %16\n\ts_nop 1" CSEL("%0", "%12", "%8") CSEL("%1", "%13", "%9") CSEL("%2", "%14", "%10") CSEL("%3", "%15", "%11")
      "\n\ts_not_b64 vcc, vcc" CSEL("%4", "%8", "%12") CSEL("%5", "%9", "%13") CSEL("%6", "%10", "%14") CSEL("%7", "%11", "%15") "\n\ts_nop 1"
      : "=&v"(c.x), "=&v"(c.y), "=&v"(c.z), "=&v"(c.w), "=&v"(d.x), "=&v"(d.y), "=&v"(d.z), "=&v"(d.w)
      : "v"(a.x), "v"(a.y), "v"(a.z), "v"(a.w), "v"(b.x), "v"(b.y), "v"(b.z), "v"(b.w), "s"(0x5555555555555555ull) : "vcc", "scc");
  a = c; b = d; (void)odd;
}
DI void pack_quad(unsigned (&p)[4][2], u32x4& a, u32x4& b, bool odd) {
  tile_xpose(p);
  a.x = p[0][0]; a.y = p[0][1]; a.z = p[1][0]; a.w = p[1][1]; b.x = p[2][0]; b.y = p[2][1]; b.z = p[3][0]; b.w = p[3][1];
  par_xchg(a, b, odd);
}
template <int AUX = 16>
DI void store_quad(brs_t r, unsigned off  , unsigned pitch, unsigned (&p)[4][2], bool odd) {
  u32x4 a, b;
  pack_quad(p, a, b, odd);
  __builtin_amdgcn_raw_buffer_store_b128(a, r, (int)off, 0, AUX); __builtin_amdgcn_raw_buffer_store_b128(b, r, (int)(off + pitch), 0, AUX);
}
DI void unpack_quad(u32x4 a, u32x4 b, unsigned (&p)[4][2], bool odd) {
  par_xchg(a, b, odd);
  p[0][0] = a.x; p[0][1] = a.y; p[1][0] = a.z; p[1][1] = a.w; p[2][0] = b.x; p[2][1] = b.y; p[3][0] = b.z; p[3][1] = b.w;
  tile_xpose(p);
}

template <int MT_, int NT_, int NS_, int BASE_, int STRIDE_>
struct Cfg { static constexpr int MT = MT_, NT = NT_, NS = NS_, BASE = BASE_, STRIDE = STRIDE_; };
template <class C> struct Frags { bf16x8 a[C::MT]; bf16x8 b[C::NT]; };

DI bf16x8 rd_bfrag(lds_t p, int hi_off) {
  s16x4 lo = __builtin_amdgcn_ds_read_tr16_b64_v4i16((LAS s16x4*)p);
  s16x4 hi = __builtin_amdgcn_ds_read_tr16_b64_v4i16((LAS s16x4*)(p + hi_off));
  return __builtin_shufflevector(lo, hi, 0, 1, 2, 3, 4, 5, 6, 7);
}
template <class C, class P>
DI void read_frags(Frags<C>& F, int ks, const unsigned (&ad)[P::NBASE + 2]) {
#pragma unroll
  for (int i = 0; i < C::NT; ++i) {
    lds_t p = (lds_t)(size_t)ad[P::base(i)];
    F.b[i] = P::narrow(i) ? rd_bfrag(p + ks * 2048, 256) : rd_bfrag(p + P::imm(i) + ks * 4096, 512);
    if (i == 0) {
#pragma unroll
      for (int m = 0; m < C::MT; ++m) F.a[m] = *(const LAS bf16x8*)((lds_t)(size_t)ad[P::NBASE + ks] + m * 2048);
    }
  }
}
template <class C, class P, int LO, int HI>
DI void mma(f32x4 (&acc)[C::MT][C::NT], const Frags<C>& F) {
#pragma unroll
  for (int i = LO; i < HI; ++i) {
    const int n = i / C::MT, m = i % C::MT;
    acc[m][n] = P::trans(n) ? __builtin_amdgcn_mfma_f32_16x16x32_bf16(F.b[n], F.a[m], acc[m][n], 0, 0, 0)
                            : __builtin_amdgcn_mfma_f32_16x16x32_bf16(F.a[m], F.b[n], acc[m][n], 0, 0, 0);
  }
}
template <int NM, int NR, int NV>
DI void sched_hint() {
  constexpr int RP = NM ? (NR > NM ? (NR + NM - 1) / NM : 1) : 1, GR = (NR + RP - 1) / RP, PV = NM > GR ? (NV + NM - GR - 1) / (NM - GR) : 0;
#pragma unroll
  for (int i = 0; i < NM; ++i) {
    __builtin_amdgcn_sched_group_barrier(0x8, 1, 0);
    if (i < GR) __builtin_amdgcn_sched_group_barrier(0x100, RP, 0);
    else if (NV) __builtin_amdgcn_sched_group_barrier(0x2, PV, 0);
  }
}
struct NoX {
  static constexpr int NX = 0;
  template <class F> DI void step(const F&) {}
  template <class F> DI void first(const F&, const F&) {}
  DI void pub() {}
};
struct Pub { brs_t r; unsigned off, tag; int ts; };
template <class C, class P, class X>
DI void consume(lds_t smem, int rs, int nt, f32x4 (&acc)[C::MT][C::NT], int arow, int lane, const unsigned (&boff)[P::NBASE], const Pub& pb, X& x) {
  constexpr int NM = C::MT * C::NT, NR = C::MT + 2 * C::NT, D = NM > NR ? NM - NR : 0, NV = P::NBASE + 2;
  const int fi = lane & 15, g = lane >> 4, f = (fi >> 1) & 7;
  const unsigned sb = (unsigned)(size_t)smem + C::BASE + rs * C::STRIDE;
  unsigned ad[NV];
#pragma unroll
  for (int i = 0; i < P::NBASE; ++i) ad[i] = sb + boff[i];
  ad[P::NBASE] = sb + (unsigned)((arow + fi) * 128 + ((g ^ f) * 16));
  ad[P::NBASE + 1] = sb + (unsigned)((arow + fi) * 128 + (((4 + g) ^ f) * 16));
  Frags<C> F0, F1;
  wait_lgkm0();
  bar();
  read_frags<C, P>(F0, 0, ad);
  __builtin_amdgcn_sched_barrier(0);
  auto rest = [&]() {
    rs = (rs + 1 == C::NS) ? 0 : rs + 1;
    const unsigned dl = rs ? (unsigned)C::STRIDE : (unsigned)(-(C::NS - 1) * C::STRIDE);
    read_frags<C, P>(F1, 1, ad);
    mma<C, P, 0, NM>(acc, F0);
    x.step(F0);
#pragma unroll
    for (int i = 0; i < NV; ++i) { ad[i] += dl; asm volatile("" : "+v"(ad[i])); }
    mma<C, P, 0, D>(acc, F1);
    sched_hint<NM + X::NX, NR, NV>();
    sched_hint<D, 0, 0>();
    __builtin_amdgcn_sched_barrier(0);
    wait_lgkm0();
  };
  rest();
  x.first(F0, F1);
#pragma unroll 1
  ALN for (int t = 0; t < nt - 1; ++t) {
    if (t == pb.ts) { wait_vm<0>(); if (lane == 0) st4(pb.r, pb.off, pb.tag); x.pub(); }
    bar();
    __builtin_amdgcn_sched_barrier(0);
    read_frags<C, P>(F0, 0, ad);
    mma<C, P, D, NM>(acc, F1);
    x.step(F1);
    sched_hint<NM - D + X::NX, NR, 0>();
    rest();
  }
  mma<C, P, D, NM>(acc, F1);
  x.step(F1);
}
#define Z64 1
typedef double f64x2 __attribute__((ext_vector_type(2)));
template <int M, int N> DI void zero64(f32x4 (&a)[M][N]) {
#pragma unroll
  for (int m = 0; m < M; ++m)
#pragma unroll
    for (int n = 0; n < N; ++n) {
      double lo, hi;
      if (Z64) { asm volatile("v_mov_b64 %0, 0" : "=v"(lo)); asm volatile("v_mov_b64 %0, 0" : "=v"(hi)); } else lo = hi = 0.0;
      a[m][n] = __builtin_bit_cast(f32x4, (f64x2){lo, hi});
    }
}
template <int M, int N> DI void zero(f32x4 (&a)[M][N]) {
#pragma unroll
  for (int m = 0; m < M; ++m)
#pragma unroll
    for (int n = 0; n < N; ++n) a[m][n] = (f32x4){0.f, 0.f, 0.f, 0.f};
}
DI unsigned voff_a(int lane, int lw, unsigned lda2) { return (unsigned)(lane >> 3) * lda2 + (unsigned)(((lane & 7) ^ (((lw & 1) * 4 + (lane >> 4)) & 7)) * 16); }
DI unsigned voff_w(int lane, int lw, unsigned ldb2) { return (unsigned)(lane >> 3) * ldb2 + (unsigned)(((lane & 7) ^ (((lane >> 4) & 1) * 2 + (lw & 1) * 4)) * 16); }
DI unsigned voff_n(int lane, unsigned ldb2) { return (unsigned)(lane >> 2) * ldb2 + (unsigned)(((lane & 3) ^ (((lane >> 5) & 1) * 2)) * 16); }
template <bool SC> DI void issue_a(rsrc_t rA, lds_t st, int lw, unsigned voff, unsigned soff  , unsigned lda2) {
#pragma unroll
  for (int i = 0; i < 4; ++i) glds16<SC>(rA, st + (lw + 4 * i) * 1024, voff, soff + (unsigned)(lw + 4 * i) * 8u * lda2);
}
template <bool SC = false> DI void issue_w(rsrc_t rB, lds_t blk, int lw, unsigned voff, unsigned soff  , unsigned ldb2) {
#pragma unroll
  for (int i = 0; i < 2; ++i) glds16<SC>(rB, blk + (lw + 4 * i) * 1024, voff, soff + (unsigned)(lw + 4 * i) * 8u * ldb2);
}
DI unsigned slow_line(const void* m) { return (3u - ((unsigned)(u64)m >> 7)) & 7u; }
struct BSrc {
  rsrc_t r; unsigned pitch, voff;
  DI void issue(lds_t blk, int lw, unsigned kt) const { issue_w<true>(r, blk, lw, voff, kt * 64u * pitch, pitch); }
};
DI BSrc mk_src(const void* m, unsigned bytes, unsigned pitch, unsigned col, unsigned vm, const char* cp, unsigned vc) {
  const bool slow = SLOWCP && ((col >> 7) & 7u) == slow_line(m);
  BSrc b; b.r = slow ? mk_rsrc(cp, 4608u * 128u) : mk_rsrc((const char*)m + col, bytes - col); b.pitch = slow ? 128u : pitch; b.voff = slow ? vc : vm; return b;
}
DI unsigned roff_w(int lane, int j) {
  const int g = lane >> 4, q = (lane >> 2) & 3, pp = lane & 3, f = ((q >> 1) & 1) | ((g & 1) << 1);
  return (unsigned)((8 * g + q) * 128 + pp * 8 + ((j ^ f) << 5));
}
DI unsigned roff_n(int lane, int j) {
  const int g = lane >> 4, q = (lane >> 2) & 3, pp = lane & 3;
  return (unsigned)(g * 512 + q * 64 + pp * 8 + ((j ^ (g & 1)) << 5));
}
DI unsigned load_tag(const char* ws) {
  unsigned e;
  asm volatile("s_load_dword %0, %1, 0x0\n\ts_waitcnt lgkmcnt(0)" : "=s"(e) : "s"(ws) : "memory");
  return e + 0x9E3779B9u;
}
#define POLLMAX (1 << 18)
DI bool tagged(u32x4 v, unsigned tag) { return __builtin_amdgcn_ballot_w64(((v.x ^ tag) | (v.y ^ tag) | (v.z ^ tag) | (v.w ^ tag)) != 0u) == 0ull; }
DI void await4(u32x4 v, const char* fp, unsigned tag) {
  if (tagged(v, tag)) return;
#pragma unroll 1
  for (int it = 0; it < POLLMAX; ++it) {
    asm volatile("global_load_dwordx4 %0, %1, off sc1\n\ts_waitcnt vmcnt(0)" : "=v"(v) : "v"(fp) : "memory");
    if (tagged(v, tag)) break;
    __builtin_amdgcn_s_sleep(1);
  }
}

#define PKL 1
#define XSL 4
typedef Cfg<4, 4, 4, 0, 36864> CA;
typedef Cfg<2, 4, 4, 0, 36864> CC;
typedef Cfg<4, 7, 3, 16384, 45056> CD;
typedef Cfg<4, 4, 4, 16384, 32768> CE;
constexpr int XTRA = 151552, LDS_BYTES = 163840;
template <bool FIRST> struct PAB {
  static constexpr int NBASE = 2;
  static constexpr bool trans(int n) { return FIRST ? n < 2 : n >= 2; }
  static constexpr bool narrow(int) { return false; }
  static constexpr int base(int i) { return i & 1; }
  static constexpr int imm(int i) { return (i >> 1) * 8192; }
};
template <int N> struct PT {
  static constexpr int NBASE = N;
  static constexpr bool trans(int) { return true; }
  static constexpr bool narrow(int i) { return i == 6; }
  static constexpr int base(int i) { return i; }
  static constexpr int imm(int) { return 0; }
};
DI bf16x8 ones8() { const u32x4 o = {ONES, ONES, ONES, ONES}; return __builtin_bit_cast(bf16x8, o); }
struct XCs1 : NoX {
  static constexpr int NX = 2;
  f32x4 cs[2];
  brs_t r; unsigned off;
  DI void pub() {
    unsigned o = off;
    asm volatile("" : "+v"(o));
#pragma unroll
    for (int i = 0; i < 4; ++i) st16(r, o + i * 1024, (u32x4){~0u, ~0u, ~0u, ~0u});
  }
  template <class F> DI void step(const F& f) {
#pragma unroll
    for (int n = 0; n < 2; ++n) cs[n] = __builtin_amdgcn_mfma_f32_16x16x32_bf16(f.b[n], ones8(), cs[n], 0, 0, 0);
  }
};
struct XCs3 : NoX {
  brs_t r; unsigned off, foff, tag; int wm, fi, lane; bool on;
  template <class F> DI void first(const F& f0, const F& f1) {
    if (!on) return;
    const f32x4 zero4 = {0.f, 0.f, 0.f, 0.f};
    f32x4 v = zero4;
    if (wm == 0) {
#pragma unroll
      for (int n = 0; n < 4; ++n) {
        const f32x4 c = __builtin_amdgcn_mfma_f32_16x16x32_bf16(f1.b[n], ones8(), __builtin_amdgcn_mfma_f32_16x16x32_bf16(f0.b[n], ones8(), zero4, 0, 0, 0), 0, 0, 0);
#pragma unroll
        for (int e = 0; e < 4; ++e) v[e] = fi == n ? c[e] : v[e];
      }
    } else {
#pragma unroll
      for (int n = 4; n < 7; ++n) {
        const f32x4 c = __builtin_amdgcn_mfma_f32_16x16x32_bf16(f1.b[n], ones8(), __builtin_amdgcn_mfma_f32_16x16x32_bf16(f0.b[n], ones8(), zero4, 0, 0, 0), 0, 0, 0);
#pragma unroll
        for (int e = 0; e < 4; ++e) v[e] = fi == n - 4 ? c[e] : v[e];
      }
    }
    asm volatile("s_waitcnt vmcnt(0)" : "+v"(v) :: "memory");
    if (lane == 0) st4(r, foff, tag);
    if (fi < 4 - wm) st16f(r, off, v);
    __builtin_amdgcn_sched_barrier(0);
  }
};
#define USTORE store_quad
#define SCA 7
#define HX 1
#define P3 0
#define P4 1
#define PO 3
#define TOUCH(p) do { if (P3 == p) gtouch(mk_rsrc(w3, D_ * F_ * 2), scr, tl); if (P4 == p) gtouch(mk_rsrc(w4, F_ * D_ * 2), scr, tl); \
    if (PO == p) gtouch(mk_rsrc(wo, D_ * D_ * 2), scr, (unsigned)((id * 4 + w) * 16 + fi) * 128u); } while (0)
#define ND1 13
#define NB1 8
#define TSB 1
#define TSU 8
#define TU2 20

DI void act(const f32x4& a4, const f32x4& c4, const f32x4& rs, unsigned (&p)[2]) {
  const f32x2 nm = {rs[0], rs[0]}, c0 = {rs[1], rs[1]}, c1 = {rs[2], rs[2]}, q = {rs[3], rs[3]};
#pragma unroll
  for (int h = 0; h < 2; ++h) {
    const f32x2 a = {a4[2 * h], a4[2 * h + 1]}, c = {c4[2 * h], c4[2 * h + 1]};
    const f32x2 t = __builtin_elementwise_fma(c, nm, a);
    const f32x2 y = t * __builtin_elementwise_fma(t * t, c1, c0);
    const f32x2 e = {__builtin_amdgcn_exp2f(y[0]), __builtin_amdgcn_exp2f(y[1])};
    const f32x2 d = __builtin_elementwise_fma(e, q, q);
    const f32x2 o = t * (f32x2){__builtin_amdgcn_rcpf(d[0]), __builtin_amdgcn_rcpf(d[1])};
    p[h] = cvt_pk(o[0], o[1]);
  }
}
#define LANE_VALUES() asm volatile("" : "+v"(lane)); const int fi = lane & 15, g = lane >> 4; const bool odd = fi & 1; \
  const unsigned qrow = (unsigned)(pm * 128 + wm * 64 + (g >> 1) * 16 + (fi & 14)), qcol = (unsigned)(pn * 64 + wn * 32 + (g & 1) * 16 + (fi & 1) * 8);   \
  const unsigned tl = (unsigned)((id * 4 + w) * 56 + (lane < 56 ? lane : 55)) * 128u;   \
  (void)odd; (void)qrow; (void)qcol; (void)tl
extern "C" __global__ __launch_bounds__(512, 2) void kf(const u16* __restrict__ x, const u16* __restrict__ wqkv, const u16* __restrict__ wo,
                                             const u16* __restrict__ w1, const u16* __restrict__ w2, const u16* __restrict__ w3,
                                             const u16* __restrict__ w4, const u16* __restrict__ gw, char* __restrict__ ws,
                                             u16* __restrict__ out) {
  extern __shared__ __attribute__((aligned(16))) char smem_[];
  lds_t smem = (lds_t)smem_;
  asm volatile("" :: "s"(x), "s"(wqkv), "s"(wo), "s"(w1), "s"(w2), "s"(w3), "s"(w4), "s"(gw), "s"(ws), "s"(out));
  const int w = __builtin_amdgcn_readfirstlane(threadIdx.x >> 6);
  int lane = threadIdx.x & 63;
  const int xcd = blockIdx.x & 7, j = blockIdx.x >> 3;
  const int pm = (xcd & 1) * 8 + (j & 7), pn = (xcd >> 1) * 4 + (j >> 3), id = pm * 16 + pn;
  const int hf = pn >> 3, pn4 = pn & 7;
  const brs_t rW = mk_brs(ws, WS_BYTES);
  const unsigned me = (unsigned)(id * 16 + (w & 3) * 4);
  const int wc0 = (pn & 1) ? 32 : 0, nc0 = (pn & 1) ? 0 : 192;
  LAS f32x4* xst = (LAS f32x4*)(smem + XTRA);
  LAS f32x4* rst = (LAS f32x4*)(smem + XTRA + 2048);
  LAS float* xcs = (LAS float*)(smem + XTRA + 4096);
  lds_t scr = smem + XTRA + 5120, zfr = smem + XTRA + 5632;
  auto iA = [&](int kt) {
    const int lw = w - 4;
    lds_t st = smem + (kt & 3) * CA::STRIDE;
    issue_a<false>(mk_rsrc(x, S_ * D_ * 2), st, lw, voff_a(lane, lw, 2048u), (unsigned)pm * (128u * 2048u) + (unsigned)kt * 128u, 2048u);
    issue_w(mk_rsrc(w1, D_ * D_ * 2), st + 16384, lw, voff_w(lane, lw, 2048u), (unsigned)kt * (64u * 2048u) + (unsigned)(pn ^ HX) * 128u, 2048u);
    issue_w(mk_rsrc(wqkv, D_ * 3 * D_ * 2), st + 24576, lw, voff_w(lane, lw, 6144u), (unsigned)kt * (64u * 6144u) + (unsigned)pn * 128u + 2048u, 6144u);
  };
  if (w >= 4) { iA(0); iA(1); iA(2); }
  int role = w;
  asm volatile("" : "+s"(role));
  if (role >= 4) {
    const int lw = w - 4, fi = lane & 15, g = lane >> 4;
    const unsigned l16 = (unsigned)lane * 16u;
    const rsrc_t rS = mk_rsrc(ws, WS_BYTES), rX = mk_rsrc(x, S_ * D_ * 2), rQ = mk_rsrc(wqkv, D_ * 3 * D_ * 2);
    const unsigned va = voff_a(lane, lw, 2048u), vw = voff_w(lane, lw, 2048u), vq = voff_w(lane, lw, 6144u);
    const unsigned aoff = (unsigned)pm * (128u * 2048u), bcol = (unsigned)pn * 128u, vh = voff_a(lane, lw, LDH), hoff = (unsigned)pm * (128u * LDH);
    lds_t pl = smem + XTRA + 8192 + lw * 1024;
    const unsigned vc = voff_w(lane, lw, 128u);
    unsigned tag;
    auto poll = [&](unsigned off) { glds16<true>(rS, pl, off, 0u); };
    auto await = [&](unsigned off) { await4(*(const LAS u32x4*)(pl + l16), ws + off, tag); };
    const BSrc sV = mk_src(wqkv, D_ * 3 * D_ * 2, 6144u, bcol + 4096u, vq, ws + WS_CPA + (pn >> 3) * 131072, vc);
    const BSrc sQ = mk_src(wqkv, D_ * 3 * D_ * 2, 6144u, bcol, vq, ws + WS_CPA + (2 + (pn >> 3)) * 131072, vc);
    auto iB = [&](int i, int kt) {
      lds_t st = smem + (i & 3) * CA::STRIDE;
      issue_a<false>(rX, st, lw, va, aoff + (unsigned)kt * 128u, 2048u);
      sV.issue(st + 16384, lw, kt);
      sQ.issue(st + 24576, lw, kt);
    };
    const unsigned f6 = FLG(F_OG) + (unsigned)(pm * 16 + hf * 8 + (lane & 7)) * 16u;
    int s = 0;
    {
      tag = load_tag(ws + WS_EPOCH);
      wait_vm<16>();

      const unsigned f0 = FLG(F_RS) + (unsigned)lw * 1024u + l16;
#pragma unroll 1
      for (; s < 13; ++s) {
        bar();
        if (s == 9) poll(f0);
        if (s == 10) { wait_vm<8>(); await(f0); }
        if (s == 11) { glds16<true>(rS, (lds_t)xst + (lw & 1) * 1024, l16, (unsigned)WS_RS + pm * 2048u + (lw & 1) * 1024u); }
        iA(s + 3);
        wait_vm<16>();
      }
    }
    {
      const int nb = pm + ((fi & 4) ? 1 : -1);
      const unsigned f1 = FLG(F_HS) + (unsigned)((((lane & 16) && (unsigned)nb < 16u) ? nb : pm) * 16 + fi) * 16u;
#pragma unroll 1
      for (; s < 13 + NB1; ++s) { bar(); iB(s + 3, s - 13); wait_vm<16>(); }
      const rsrc_t rH = mk_rsrc(ws + WS_HS, S_ * LDH);
      const BSrc s2 = mk_src(w2, D_ * D_ * 2, 2048u, bcol, vw, ws + WS_CPA + (4 + (pn >> 3)) * 131072, vc);
      auto iCa = [&](int i, int kt) { issue_a<SCA & 1>(rH, smem + (i & 3) * CA::STRIDE, lw, vh, hoff + (unsigned)kt * 128u, LDH); };
      auto iCb = [&](int i, int kt) { s2.issue(smem + (i & 3) * CA::STRIDE + 16384, lw, kt); };
      bar(); iCb(s + 3, 0); wait_vm<10>(); ++s;
      bar(); iCb(s + 3, 1); poll(f1); wait_vm<5>(); ++s;
      bar(); wait_vm<0>(); await(f1); iCa(s + 1, 0); iCa(s + 2, 1); iCa(s + 3, 2); iCb(s + 3, 2); wait_vm<10>(); ++s;
#pragma unroll 1
      for (; s < 29 + NB1; ++s) { bar(); iCa(s + 3, s - 13 - NB1); iCb(s + 3, s - 13 - NB1); wait_vm<12>(); }
      bar(); iB(s + 3, NB1); wait_vm<14>(); ++s;
#pragma unroll 1
      for (; s < 45; ++s) { bar(); iB(s + 3, s - 29); wait_vm<16>(); }
    }
    {
      const rsrc_t rA = mk_rsrc(ws + WS_X1, S_ * LDH), rB = mk_rsrc(w3, D_ * F_ * 2);
      const unsigned vb = voff_w(lane, lw, 7168u), vn = voff_n(lane, 7168u);
      const unsigned wcol = (unsigned)(pn * 224 + wc0) * 2u, ncol = (unsigned)(pn * 224 + nc0) * 2u + (unsigned)lw * (16u * 7168u);
      int slot = 0;
      auto iDa = [&](int t, int sl) { issue_a<(SCA & 2) != 0>(rA, smem + CD::BASE + sl * CD::STRIDE, lw, vh, hoff + ((unsigned)(t + pm) & 15u) * 128u, LDH); };
      auto iDb = [&](int t, int sl) {
        lds_t st = smem + CD::BASE + sl * CD::STRIDE;
        const unsigned kb = ((unsigned)(t + pm) & 15u) * (64u * 7168u);
#pragma unroll
        for (int b = 0; b < 3; ++b) issue_w(rB, st + 16384 + b * 8192, lw, vb, kb + wcol + b * 128u, 7168u);
        glds16<false>(rB, st + 16384 + 24576 + lw * 1024, vn, kb + ncol);
      };
      auto iD = [&](int t) { iDa(t, slot); iDb(t, slot); slot = slot == 2 ? 0 : slot + 1; };
      const unsigned f2 = FLG(F_X1) + (unsigned)(pm * 16 + fi) * 16u;
      bar(); wait_vm<8>();
      bar(); poll(f2); iDb(0, 0); wait_vm<8>();
      bar(); wait_vm<7>(); await(f2); iDa(0, 0); iDa(1, 1); iDb(1, 1); wait_vm<11>();
      slot = 2;
      const int kh = id >> 3, kTU = (id >> 1) & 3, khalf = id & 1;
      const unsigned f3 = FLG(F_KVP) + (unsigned)(fi * 16 + (kh >> 1)) * 16u;
      const unsigned f4 = (lane & 16) ? FLG(F_CS3) + (unsigned)(fi * 16 + pn) * 16u : FLG(F_KVF) + (unsigned)(pn * 16 + fi) * 16u;
      lds_t zone = smem + lw * 4096;
      auto sum4 = [&](lds_t p) { f32x4 v = *(const LAS f32x4*)(p + l16);
#pragma unroll
        for (int i = 1; i < 4; ++i) v += *(const LAS f32x4*)(p + i * 1024 + l16);
        return v; };
      bar(); iD(2);
#pragma unroll
      for (int i = 0; i < 4; ++i) glds16<true>(rS, zone + i * 1024, (unsigned)g * 1024u + fi * 16u, (unsigned)WS_ST + (pm * 16 + i * 4) * 1024u + lw * 256u);
      wait_vm<15>();
      bar(); iD(3); wait_vm<11>();
      asm volatile("s_waitcnt lgkmcnt(0)");
      bar(); iD(4);
      f32x4 v = sum4(zone);
#pragma unroll
      for (int e = 0; e < 4; ++e) v[e] = sum_g(v[e]);
      asm volatile("" : "+v"(v));
      wait_vm<11>();
      bar(); iD(5);
      {
        const int lr = lw * 32 + fi * 2 + (g & 1);
        const float omg = xst[lr][3];
        const float mean = ((g & 1) ? v[2] : v[0]) * (1.f / 1024.f);
        const float var = fmaxf(((g & 1) ? v[3] : v[1]) * (1.f / 1024.f) - mean * mean, 0.f);
        const float rstd = __builtin_amdgcn_rsqf(var + 1e-5f);
        if (g < 2) rst[lr] = (f32x4){-mean, -2.30220819814f * rstd, -0.10294323970f * rstd * rstd * rstd, fminf(__builtin_amdgcn_rcpf(rstd * omg), 1e30f)};
      }
      poll(f3);
      wait_vm<12>();
      bar(); iD(6); wait_vm<11>(); await(f3);
#pragma unroll
      for (int i = 0; i < 4; ++i)
        glds16<true>(rS, zone + i * 1024, (unsigned)(lane >> 3) * 4096u + (lane & 7) * 16u, (unsigned)WS_KVP + ((kh * 32 + 8 * i) * 4 + kTU) * 1024u + khalf * 512u + lw * 128u);
      bar(); iD(7); wait_vm<11>();
      bar(); iD(8);
      {
        v = sum4(zone);
#pragma unroll
        for (int e = 0; e < 4; ++e) v[e] = sum_r8(v[e]);
        u32x2 o; o.x = cvt_pk(v[0], v[1]); o.y = cvt_pk(v[2], v[3]);
        if (lane < 8) st8(rW, (unsigned)WS_KVF + (kh * 2 + (kTU & 1)) * 1024 + (kTU >> 1) * 512 + (khalf * 32 + lw * 8 + lane) * 8, o);
      }
      wait_vm<12>();
      bar(); iD(9); wait_vm<11>();
      if (lane == 0) st4(rW, FLG(F_KVF) + me, tag);
      bar(); iD(10); wait_vm<12>();
      bar(); iD(11); poll(f4); wait_vm<12>();
      bar(); iD(12); wait_vm<11>(); await(f4);
      if (lw < 2) {
#pragma unroll
        for (int i = 0; i < 4; ++i) glds16<true>(rS, smem + 4096 + (lw * 4 + i) * 1024, l16, (unsigned)WS_ZP + pn * 8192u + (lw * 4 + i) * 1024u);
      } else {
#pragma unroll
        for (int i = 0; i < 2; ++i) glds16<true>(rS, smem + (lw * 2 - 4 + i) * 1024, l16, (unsigned)WS_KVF + (pn * 4 + lw * 2 - 4 + i) * 1024u);
      }
      bar(); iD(13); wait_vm<11>();
      if (lw < 2) {
        v = sum4(smem + 4096 + lw * 4096);
#pragma unroll
        for (int e = 0; e < 4; ++e) v[e] = sum_r8(v[e]);
        u32x2 o; o.x = cvt_pk(v[0], v[1]); o.y = cvt_pk(v[2], v[3]);
        if (lane < 8) *(LAS u32x2*)(zfr + lw * 64 + (lane & 3) * 16 + (lane >> 2) * 8) = o;
      }
      wait_lgkm0();
      bar(); iD(14); wait_vm<11>();
      bar(); iD(15); wait_vm<11>();
      bar();
      lds_t st = smem + CD::BASE + CD::STRIDE + lw * 4096;
#pragma unroll
      for (int i = 0; i < 4; ++i) glds16<true>(rS, st + i * 1024, (unsigned)(g * F_ * 4 + (fi < 14 ? fi : 13) * 16), (unsigned)WS_CS3 + (i * 4 * F_ + pn * 224 + lw * 56) * 4u);
      wait_vm<4>();
      bar();
      wait_vm<0>();
      f32x4 c = sum4(st);
#pragma unroll
      for (int e = 0; e < 4; ++e) c[e] = sum_g(c[e]);
      if (lane < 14) *(LAS f32x4*)(xcs + lw * 56 + lane * 4) = c;
      wait_lgkm0();
      bar();
    }
    {
      const rsrc_t rA = mk_rsrc(ws + WS_AU, S_ * LDAU * 2);
      const unsigned vu = voff_a(lane, lw, LDAU * 2u), uoff = (unsigned)pm * 128u * (LDAU * 2u), ecol = (unsigned)pn4 * 256u;
      const char* cp = ws + WS_CPE + (hf * 576 + (pn4 >> 2) * 288) * 1024;
      BSrc s0 = mk_src(wo, D_ * D_ * 2, 2048u, ecol, vw, cp - hf * 65536, vc), s1 = mk_src(wo, D_ * D_ * 2, 2048u, ecol + 128u, vw, cp - hf * 65536, vc);
      auto kb_of = [&](int t) {
        const int r = t - 10, s1 = t < 10 ? 2 * pn4 + t - 8 : r + (r >= 2 * pn4 ? 2 : 0), q = t - 24, q3 = (q * 11) >> 5, qr = q - 3 * q3;
        return t < 8 ? hf * 8 + t : hf * 28 + (t < 24 ? 7 * (s1 >> 2) + (s1 & 1) + (s1 & 2) * 2 : 7 * q3 + 2 + qr + (qr & 2));
      };
      auto eA = [&](int t) {
        lds_t st = smem + CE::BASE + (t & 3) * CE::STRIDE;
        const unsigned so = uoff + (unsigned)(kb_of(t) + (t < 8 ? 0 : 16)) * 128u;
#pragma unroll
        for (int i = 0; i < 4; ++i) glds16<true>(rA, st + (lw + 4 * i) * 1024, vu, so + (unsigned)(lw + 4 * (i ^ hf)) * (8u * LDAU * 2u));
      };
      auto eB = [&](int t) {
        lds_t st = smem + CE::BASE + (t & 3) * CE::STRIDE;
        const unsigned kb = (unsigned)kb_of(t);
        s0.issue(st + 16384, lw, kb);
        s1.issue(st + 24576, lw, kb);
      };
      const unsigned f7 = f6 + (FLG(F_U) - FLG(F_OG));
      const bool late = s0.pitch != s1.pitch;
      if (!late) { eB(0); eB(1); eB(2); }
      await4((u32x4){~tag, 0u, 0u, 0u}, ws + f6, tag);
      if (late) { eB(0); eB(1); eB(2); }
      eA(0); eA(1); eA(2);
      wait_vm<8>();
      bar(); eA(3); eB(3); wait_vm<12>();
      int t = 4;
#pragma unroll 1
      for (; t < 8; ++t) { bar(); eA(t); eB(t); wait_vm<16>(); }
      s0 = mk_src(w4, F_ * D_ * 2, 2048u, ecol, vw, cp + 65536 - hf * 229376, vc); s1 = mk_src(w4, F_ * D_ * 2, 2048u, ecol + 128u, vw, cp + 65536 - hf * 229376, vc);
      bar(); poll(f7 + (FLG(F_U1) - FLG(F_U))); eA(8); eB(8); wait_vm<17>();
      bar(); eA(9); eB(9); wait_vm<16>(); await(f7 + (FLG(F_U1) - FLG(F_U)));
      t = 10;
#pragma unroll 1
      for (; t < TU2; ++t) { bar(); eA(t); eB(t); wait_vm<16>(); }
      bar(); poll(f7); eA(t); eB(t); wait_vm<17>(); ++t;
      bar(); eA(t); eB(t); wait_vm<16>(); await(f7); ++t;
#pragma unroll 1
      for (; t < 36; ++t) { bar(); eA(t); eB(t); wait_vm<16>(); }
      bar(); wait_vm<8>();
      bar(); wait_vm<0>();
      bar();
    }
    return;
  }
  const int wm = w >> 1, wn = w & 1;
  unsigned tag, ct = 0;
  {
    const int hl = lane & 31, r = pm * 128 + pn * 8 + w * 2 + (lane >> 5);
    u32x4 xv[4], gv[4];
#pragma unroll
    for (int i = 0; i < 4; ++i) xv[i] = *(const u32x4*)(x + (size_t)r * D_ + (i * 32 + hl) * 8);
#pragma unroll
    for (int i = 0; i < 4; ++i) gv[i] = *(const u32x4*)(gw + (i * 32 + hl) * 8);
    const int cu = id * 3 + w;
    u32x4 cv;
    if (SLOWCP && w < 3) {
      const unsigned crow = (unsigned)((cu & 127) * 8 + (lane >> 3)), cc = (unsigned)(cu & 128) * 8u + (lane & 7) * 16u;
      cv = *(const u32x4*)(cu >= 512 ? (const char*)w2 + crow * 2048u + slow_line(w2) * 128u + cc : (const char*)wqkv + crow * 6144u + (cu < 256 ? 4096u : 0u) + slow_line(wqkv) * 128u + cc);
    }
    const rsrc_t rQ = mk_rsrc(wqkv, D_ * 3 * D_ * 2);
    const unsigned line = (unsigned)(id * 64 + lane), lq = (line >> 4) * 6144u + (line & 15) * 128u;
    if (w == 0) gtouch(mk_rsrc(w1, D_ * D_ * 2), scr, line * 128u);
    else if (w == 1) gtouch(rQ, scr, lq + 2048u);
    else if (w == 3 && j < 8) CODE_TOUCH("kf", "kend", j * 64 + lane, ct);
    if (w == 2) gtouch<true>(mk_rsrc(ws, WS_BYTES), scr, lane < 10 ? FLG(0) + (unsigned)(lane * 4096 + id * 16) : (unsigned)WS_RS + (unsigned)(pm * 128 + pn * 8) * 16u);
    tag = load_tag(ws + WS_EPOCH);
    float s = 0.f, a = 0.f;
#pragma unroll
    for (int i = 0; i < 4; ++i)
#pragma unroll
      for (int e = 0; e < 4; ++e) {
        s = dot2(xv[i][e], ONES, s);
        a = dot2(cvt_pk(bf_lo(xv[i][e]) * bf_lo(gv[i][e]), bf_hi(xv[i][e]) * bf_hi(gv[i][e])), ONES, a);
      }
    s = sum_h32(s); a = sum_h32(a);
    const float mean = s * (1.f / 1024.f);
    float q = 0.f;
#pragma unroll
    for (int i = 0; i < 4; ++i)
#pragma unroll
      for (int e = 0; e < 4; ++e) {
        float d = bf_lo(xv[i][e]) - mean; q = __builtin_fmaf(d, d, q);
        d = bf_hi(xv[i][e]) - mean; q = __builtin_fmaf(d, d, q);
      }
    q = sum_h32(q);
    const float rstd = 1.f / sqrtf(q * (1.f / 1024.f) + 1e-5f);
    const float gg = rbf(1.f / (1.f + __expf(-rbf(a))));
    if (hl == 0) st16f(rW, (unsigned)WS_RS + r * 16, (f32x4){rstd, -mean * rstd, gg, rbf(1.f - gg)});
    if (SLOWCP && w < 3) st16(rW, (unsigned)WS_CPA + cu * 1024 + lane * 16, cv);
    if (w == 0) gtouch(rQ, scr, lq + 4096u);
    else if (w == 1) gtouch(rQ, scr, lq);
    else if (w == 2) gtouch(mk_rsrc(w2, D_ * D_ * 2), scr, line * 128u);
  }

  NoX nox;
  unsigned boff2[2];
  boff2[0] = 16384 + roff_w(lane, 2 * wn); boff2[1] = 16384 + roff_w(lane, 2 * wn + 1);
  u32x4 pk[2][2], pq[4];
  f32x4 ka[4][2];
  {
    f32x4 acc[4][4];
    zero(acc);
    XCs1 xc; xc.cs[0] = acc[0][0]; xc.cs[1] = acc[0][0]; xc.r = rW; xc.off = (unsigned)WS_XCH + ((id ^ 8) * 4 + w) * 4096 + lane * 16;
    consume<CA, PAB<true>>(smem, 0, 16, acc, wm * 64, lane, boff2, Pub{rW, FLG(F_RS) + me, tag, 3}, xc);
    asm volatile("" ::"v"(ct));
    LANE_VALUES();
    TOUCH(1);
    const int cg = pn >> 2, sh = (cg & 2) ? 2 : 1, dl = (cg & 1) ? -sh : sh;
    const unsigned hs = (unsigned)WS_HS + ((qcol * 2) ^ (HX * 128u));
#pragma unroll
    for (int mp = 0; mp < 2; ++mp) {
      unsigned p[4][2];
#pragma unroll
      for (int i = 0; i < 4; ++i) {
        const int m = mp * 2 + (i >> 1), n = i & 1;
        const f32x4 rs = xst[wm * 64 + m * 16 + fi];
        float h[4];
#pragma unroll
        for (int r = 0; r < 4; ++r) h[r] = gelu_t(__builtin_fmaf(acc[m][n][r], rs[0], rs[1] * xc.cs[n][r]));
        p[i][0] = cvt_pk(h[0], h[1]); p[i][1] = cvt_pk(h[2], h[3]);
      }
      u32x4 a, b;
      pack_quad(p, a, b, odd);
      const int d0 = (int)qrow + mp * 32 + dl, d1 = d0 + 1;
      if ((unsigned)d0 < (unsigned)S_) st16(rW, hs + d0 * LDH, a);
      if ((unsigned)d1 < (unsigned)S_) st16(rW, hs + d1 * LDH, b);
    }
    if (dl > 0 ? (pm == 0 && wm == 0) : (pm == 15 && wm == 1)) {
      if (g < 2 && (fi >> 1) < sh) { const int zr = dl > 0 ? (fi >> 1) : S_ - 1 - (fi >> 1); const u32x4 zz = {0u, 0u, 0u, 0u}; st16(rW, hs + zr * LDH, zz); }
    }
#pragma unroll
    for (int m = 0; m < 4; ++m) { ka[m][0] = acc[m][2]; ka[m][1] = acc[m][3]; }
  }
  {
    f32x4 acc[4][4];
    zero64(acc);
#pragma unroll 1
    for (int ph = 0; ph < 2; ++ph) {
      consume<CA, PAB<false>>(smem, ph ? (NB1 & 3) : 0, ph ? 16 - NB1 : NB1, acc, wm * 64, lane, boff2, Pub{rW, FLG(ph ? F_X1 : F_HS) + me, tag, TSB}, nox);
      if (ph) break;
      LANE_VALUES();
      const unsigned eo = (unsigned)((pm * 128 + w * 32 + (fi & 14)) * D_ + pn * 64 + g * 16 + (fi & 1) * 8) * 2;
      u32x4 xr[2][2];
#pragma unroll
      for (int i = 0; i < 4; ++i) xr[i >> 1][i & 1] = *(const u32x4*)((const char*)x + eo + ((i >> 1) * 16 + (i & 1)) * 2048);
      const int cu = (pm * 8 + pn4) * 4 + w;
      u32x4 cv[2];
      auto csrc = [&](int U) {
        const int st = U >= 288, rb = U - st * 288;
        const unsigned o = (unsigned)(lane >> 3) * 2048u + st * 1024u + (lane & 7) * 16u;
        return *(const u32x4*)(rb < 64 ? (const char*)wo + (hf * 64 + rb) * 16384 + slow_line(wo) * 128u + o : (const char*)w4 + (hf * 224 + rb - 64) * 16384 + slow_line(w4) * 128u + o);
      };
      TOUCH(2);
      if (SLOWCP) { cv[0] = csrc(cu); if ((cu & 7) == 0) cv[1] = csrc(512 + (cu >> 3)); }
#pragma unroll
      for (int mp = 0; mp < 2; ++mp)
#pragma unroll
        for (int T = 0; T < 2; ++T) { if (PKL) asm volatile("" : "+v"(ka[2 * mp][T]), "+v"(ka[2 * mp + 1][T])); pk[mp][T] = phi_pk(ka[2 * mp][T], ka[2 * mp + 1][T]); asm volatile("" : "+v"(pk[mp][T])); }
      unsigned boff4[4];
#pragma unroll
      for (int i = 0; i < 4; ++i) boff4[i] = 16384 + roff_w(lane, i);
      f32x4 ac[2][4];
      zero(ac);
      consume<CC, PT<4>>(smem, NB1 & 3, 16, ac, w * 32, lane, boff4, Pub{rW, 0u, 0u, -1}, nox);
      TOUCH(3);
#pragma unroll
      for (int m = 0; m < 2; ++m) {
        float s1 = 0.f, s2 = 0.f;
        unsigned p[4][2];
        unpack_quad(xr[m][0], xr[m][1], p, odd);
#pragma unroll
        for (int n = 0; n < 4; ++n) {
          p[n][0] = cvt_pk(ac[m][n][0] + bf_lo(p[n][0]), ac[m][n][1] + bf_hi(p[n][0]));
          p[n][1] = cvt_pk(ac[m][n][2] + bf_lo(p[n][1]), ac[m][n][3] + bf_hi(p[n][1]));
          s1 = dot2(p[n][0], ONES, s1); s1 = dot2(p[n][1], ONES, s1);
          s2 = dot2(p[n][0], p[n][0], s2); s2 = dot2(p[n][1], p[n][1], s2);
        }
        store_quad(rW, (unsigned)WS_X1 + eo + (unsigned)(pm * 128 + w * 32 + (fi & 14)) * (LDH - 2048u) + m * 16 * LDH, LDH, p, odd);
        s1 = sum_g(s1); s2 = sum_g(s2);
        if (g == 0) { u32x2 v; v.x = __float_as_uint(s1); v.y = __float_as_uint(s2); st8(rW, (unsigned)WS_ST + id * 1024 + (w * 32 + m * 16 + fi) * 8, v); }
      }
      if (SLOWCP) {
        st16(rW, (unsigned)WS_CPE + (hf * 576 + cu) * 1024 + lane * 16, cv[0]);
        if ((cu & 7) == 0) st16(rW, (unsigned)WS_CPE + (hf * 576 + 512 + (cu >> 3)) * 1024 + lane * 16, cv[1]);
      }
    }
    LANE_VALUES();
    TOUCH(4);
    const int head = pn * 2 + wn, part = pm * 2 + wm;
    f32x4 kv[2][2], z[2];
    zero(kv);
    z[0] = kv[0][0]; z[1] = kv[0][0];
#pragma unroll
    for (int mp = 0; mp < 2; ++mp) {
      u32x4 vv[2];
#pragma unroll
      for (int U = 0; U < 2; ++U) {
        const f32x4 c = acc[2 * mp][U], d = acc[2 * mp + 1][U];
        vv[U].x = cvt_pk(c[0], c[1]); vv[U].y = cvt_pk(c[2], c[3]); vv[U].z = cvt_pk(d[0], d[1]); vv[U].w = cvt_pk(d[2], d[3]);
      }
#pragma unroll
      for (int T = 0; T < 2; ++T) {
#pragma unroll
        for (int U = 0; U < 2; ++U)
          kv[T][U] = __builtin_amdgcn_mfma_f32_16x16x32_bf16(__builtin_bit_cast(bf16x8, pk[mp][T]), __builtin_bit_cast(bf16x8, vv[U]), kv[T][U], 0, 0, 0);
        z[T] = __builtin_amdgcn_mfma_f32_16x16x32_bf16(__builtin_bit_cast(bf16x8, pk[mp][T]), ones8(), z[T], 0, 0, 0);
      }
    }
    const unsigned kp = (unsigned)WS_KVP + (head * 32 + part) * 4096 + lane * 16;
#pragma unroll
    for (int T = 0; T < 2; ++T)
#pragma unroll
      for (int U = 0; U < 2; ++U) st16f(rW, kp + (T * 2 + U) * 1024, kv[T][U]);
    if (fi == 0) {
#pragma unroll
      for (int T = 0; T < 2; ++T) st16f(rW, (unsigned)WS_ZP + (head * 32 + part) * 128 + T * 64 + g * 16, z[T]);
    }
#pragma unroll
    for (int m = 0; m < 4; ++m) { pq[m] = phi_pk(acc[m][2], acc[m][3]); asm volatile("" : "+v"(pq[m])); }
    TOUCH(5);
  }
  {
    unsigned boff[7];
#pragma unroll
    for (int i = 0; i < 4; ++i) boff[i] = 16384 + wn * 8192 + roff_w(lane, i);
    boff[4] = 16384 + 16384 + roff_w(lane, 2 * wn); boff[5] = 16384 + 16384 + roff_w(lane, 2 * wn + 1);
    boff[6] = 16384 + 24576 + roff_n(lane, wn);
    f32x4 acc[4][7];
    zero64(acc);
    XCs3 x3; x3.r = rW; x3.wm = wm; x3.fi = lane & 15; x3.lane = lane; x3.foff = FLG(F_KVP) + me; x3.tag = tag;
    x3.off = (unsigned)WS_CS3 + (unsigned)(pm * F_ + pn * 224 + (lane >> 4) * 4 + (wm == 0 ? wc0 + wn * 64 + x3.fi * 16 : x3.fi < 2 ? wc0 + 128 + wn * 32 + x3.fi * 16 : nc0 + wn * 16)) * 4u;
#pragma unroll 1
    for (int ph = 0; ph < 2; ++ph) {
      x3.on = !ph;
      consume<CD, PT<7>>(smem, ph ? ND1 % 3 : 0, ph ? 16 - ND1 : ND1, acc, wm * 64, lane, boff, Pub{rW, FLG(ph ? F_OG : F_CS3) + me, tag, ph ? 15 - ND1 - 1 : 2}, x3);
      if (ph) break;
      LANE_VALUES();
      unsigned p2[4][2];
      {
        bf16x8 kf[2];
#pragma unroll
        for (int U = 0; U < 2; ++U) {
          lds_t kp = smem + wn * 2048 + U * 1024 + lane * 8;
          kf[U] = __builtin_shufflevector(*(const LAS s16x4*)kp, *(const LAS s16x4*)(kp + 512), 0, 1, 2, 3, 4, 5, 6, 7);
        }
        const bf16x8 zf = *(const LAS bf16x8*)(zfr + wn * 64 + g * 16);
#pragma unroll
        for (int m = 0; m < 4; ++m) {
          const float gg = xst[wm * 64 + m * 16 + fi][2];
          const f32x4 zero4 = {0.f, 0.f, 0.f, 0.f};
          const bf16x8 q = __builtin_bit_cast(bf16x8, pq[m]);
          const f32x4 n0 = __builtin_amdgcn_mfma_f32_16x16x32_bf16(kf[0], q, zero4, 0, 0, 0);
          const f32x4 n1 = __builtin_amdgcn_mfma_f32_16x16x32_bf16(kf[1], q, zero4, 0, 0, 0);
          const f32x4 dn = __builtin_amdgcn_mfma_f32_16x16x32_bf16(zf, q, zero4, 0, 0, 0);
          const float sc = gg * __builtin_amdgcn_rcpf(dn[0] + 1e-6f);
          p2[(m & 1) * 2][0] = cvt_pk(n0[0] * sc, n0[1] * sc); p2[(m & 1) * 2][1] = cvt_pk(n0[2] * sc, n0[3] * sc);
          p2[(m & 1) * 2 + 1][0] = cvt_pk(n1[0] * sc, n1[1] * sc); p2[(m & 1) * 2 + 1][1] = cvt_pk(n1[2] * sc, n1[3] * sc);
          if (m & 1) store_quad(rW, (unsigned)WS_AU + ((qrow + (m - 1) * 16) * LDAU + qcol) * 2, LDAU * 2, p2, odd);
        }
      }
    }
    LANE_VALUES();
    lds_t xch = smem + CE::BASE + 3 * CE::STRIDE;
#pragma unroll
    for (int i = 0; i < 2; ++i) {
      f32x4 snd;
#pragma unroll
      for (int r = 0; r < 4; ++r) snd[r] = wn ? acc[i][6][r] : acc[2 + i][6][r];
      *(LAS f32x4*)(xch + (w * 2 + i) * 1024 + lane * 16) = snd;
    }
    wait_lgkm0();
    bar();
    unsigned p2[4][2];
    __builtin_amdgcn_sched_barrier(0);
    f32x4 cs[8], an[4];
#pragma unroll
    for (int i = 0; i < 2; ++i) {
      const f32x4 o = *(const LAS f32x4*)(xch + ((w ^ 1) * 2 + i) * 1024 + lane * 16);
#pragma unroll
      for (int r = 0; r < 4; ++r) { const float own = wn ? acc[2 + i][6][r] : acc[i][6][r]; an[2 * i][r] = wn ? o[r] : own; an[2 * i + 1][r] = wn ? own : o[r]; }
    }
#pragma unroll
    for (int n = 0; n < 8; ++n) {
      const int c0 = (n < 4 ? wc0 + wn * 64 + n * 16 : n < 6 ? wc0 + 128 + wn * 32 + (n - 4) * 16 : nc0 + (n - 6) * 16) + g * 4;
      cs[n] = *(const LAS f32x4*)(xcs + c0);
    }
    const unsigned urow = (unsigned)WS_AU + ((pm * 128 + wm * 64 + (fi & 14)) * LDAU + 1024 + pn * 224 + (fi & 1) * 8) * 2;
    [[maybe_unused]] const unsigned u2 = urow + ((g >> 1) * 16 * LDAU + (g & 1) * 16) * 2;
#pragma unroll
    for (int m = 0; m < 4; ++m) {
      const f32x4 rs = rst[wm * 64 + m * 16 + fi];
      unsigned p[4][2];
#pragma unroll
      for (int n = 0; n < 4; ++n) act(acc[m][n], cs[n], rs, p[n]);
      USTORE(rW, urow + (m * 16 * LDAU + wc0 + wn * 64 + g * 16) * 2, LDAU * 2, p, odd);
    }
    __builtin_amdgcn_sched_barrier(0);
#pragma unroll
    for (int m = 0; m < 4; ++m) {
      const f32x4 rs = rst[wm * 64 + m * 16 + fi];
      act(acc[m][4], cs[4], rs, p2[(m & 1) * 2]); act(acc[m][5], cs[5], rs, p2[(m & 1) * 2 + 1]);
      if (m & 1) USTORE(rW, u2 + ((m - 1) * 16 * LDAU + wc0 + 128 + wn * 32) * 2, LDAU * 2, p2, odd);
    }
#pragma unroll
    for (int i = 0; i < 2; ++i) {
      const f32x4 rs = rst[wm * 64 + (wn * 2 + i) * 16 + fi];
      act(an[2 * i], cs[6], rs, p2[2 * i]); act(an[2 * i + 1], cs[7], rs, p2[2 * i + 1]);
    }
    USTORE(rW, u2 + (wn * 32 * LDAU + nc0) * 2, LDAU * 2, p2, odd);
    wait_vm<6>();
    if (lane == 0) st4(rW, FLG(F_U1) + me, tag);
  }
  LANE_VALUES();
  const int row0 = wm * 64 + hf * 32;
  const unsigned ecl = (unsigned)(pn4 * 128 + wn * 64 + g * 16 + (fi & 1) * 8) * 2;
  const unsigned eoff = (unsigned)(pm * 128 + row0 + (fi & 14)) * 2048u + ecl;
  u32x4 xr[2][2];
#pragma unroll
  for (int i = 0; i < 4; ++i) xr[i >> 1][i & 1] = ld16(rW, (unsigned)WS_X1 + (unsigned)(pm * 128 + row0 + (fi & 14) + (i >> 1) * 16 + (i & 1)) * LDH + ecl);
  unsigned boff[4];
#pragma unroll
  for (int i = 0; i < 4; ++i) boff[i] = 16384 + wn * 8192 + roff_w(lane, i);
  f32x4 acc[4][4];
  zero(acc);
  consume<CE, PT<4>>(smem, 0, 36, acc, wm * 64, lane, boff, Pub{rW, FLG(F_U) + me, tag, TSU}, nox);
  {
    const unsigned ob = (unsigned)WS_XCH + (id * 4 + w) * 4096 + lane * 16;
#pragma unroll
    for (int i = 0; i < 4; ++i) {
      const f32x4 a = acc[2 + (i >> 1)][(i & 1) * 2], b = acc[2 + (i >> 1)][(i & 1) * 2 + 1];
      u32x4 v = {cvt_pk(a[0], a[1]), cvt_pk(a[2], a[3]), cvt_pk(b[0], b[1]), cvt_pk(b[2], b[3])};
      v = __builtin_elementwise_min(v, (u32x4){~1u, ~1u, ~1u, ~1u});
      st16(rW, ob + i * 1024, v);
    }
    __builtin_amdgcn_sched_barrier(0);
#pragma unroll
    for (int m = 0; m < 2; ++m) {
      unsigned p[4][2];
      unpack_quad(xr[m][0], xr[m][1], p, odd);
      const float q = xst[row0 + m * 16 + fi][3];
#pragma unroll
      for (int n = 0; n < 4; ++n) {
        acc[m][n][0] = __builtin_fmaf(q, bf_lo(p[n][0]), acc[m][n][0]); acc[m][n][1] = __builtin_fmaf(q, bf_hi(p[n][0]), acc[m][n][1]);
        acc[m][n][2] = __builtin_fmaf(q, bf_lo(p[n][1]), acc[m][n][2]); acc[m][n][3] = __builtin_fmaf(q, bf_hi(p[n][1]), acc[m][n][3]);
        asm volatile("" : "+v"(acc[m][n]));
      }
    }
    wait_vm<0>();
    const unsigned ib = ob ^ (8u * 16384u);
    u32x4 o[4];
#pragma unroll 1
    for (int it = 0; it < POLLMAX; ++it) {
#pragma unroll
      for (int i = 0; i < 4; ++i) o[i] = ld16(rW, ib + i * 1024);
      u32x4 mx = __builtin_elementwise_max(__builtin_elementwise_max(o[0], o[1]), __builtin_elementwise_max(o[2], o[3]));
      if (__builtin_amdgcn_ballot_w64(max(max(mx.x, mx.y), max(mx.z, mx.w)) == ~0u) == 0ull) break;
      asm volatile("" ::: "memory");
      __builtin_amdgcn_s_sleep(XSL);
    }
    if (id == 0 && threadIdx.x == 0) st4(rW, (unsigned)WS_EPOCH, tag);
    const brs_t rO = mk_brs(out, S_ * D_ * 2);
#pragma unroll
    for (int m = 0; m < 2; ++m) {
      unsigned p[4][2];
#pragma unroll
      for (int n = 0; n < 4; ++n) {
        const unsigned b0 = o[m * 2 + (n >> 1)][(n & 1) * 2], b1 = o[m * 2 + (n >> 1)][(n & 1) * 2 + 1];
        p[n][0] = cvt_pk(acc[m][n][0] + bf_lo(b0), acc[m][n][1] + bf_hi(b0));
        p[n][1] = cvt_pk(acc[m][n][2] + bf_lo(b1), acc[m][n][3] + bf_hi(b1));
      }
      store_quad(rO, eoff + m * 32768, 2048, p, odd);
    }
  }
}

extern "C" __global__ void kend() {}

extern "C" void kernel_launch(void* const* d_in, const int* in_sizes, int n_in, void* d_out, int out_size, void* d_ws, size_t ws_size,
                              hipStream_t stream) {
  (void)hipFuncSetAttribute((const void*)kf, hipFuncAttributeMaxDynamicSharedMemorySize, LDS_BYTES);
  kf<<<256, 512, LDS_BYTES, stream>>>((const u16*)d_in[0], (const u16*)d_in[1], (const u16*)d_in[2], (const u16*)d_in[4], (const u16*)d_in[5],
                                      (const u16*)d_in[6], (const u16*)d_in[7], (const u16*)d_in[8], (char*)d_ws, (u16*)d_out);
}
```
